```python
import torch

def _softmax(x, axis=-1):
    return torch.softmax(x, dim=axis)
def _gelu(x):
    return torch.nn.functional.gelu(x, approximate="tanh")
def _silu(x):
    return torch.nn.functional.silu(x)
def _sigmoid(x):
    return torch.sigmoid(x)
def _layernorm(x, axis=-1, eps=1e-5):
    m = x.mean(dim=axis, keepdim=True)
    v = x.var(dim=axis, keepdim=True, unbiased=False)
    return (x - m) / torch.sqrt(v + eps)
def _scatter_add(tgt, idx, src, axis=0):
    return tgt.clone().index_add_(axis, idx, src)
def _segment_sum(src, seg, n):
    out = torch.zeros((n,) + src.shape[1:], dtype=src.dtype)
    return out.index_add_(0, seg, src)
def _pad(x, pw):
    flat = tuple(v for pair in reversed(pw) for v in pair)
    return torch.nn.functional.pad(x, flat)

kernel_name = 'gated_mix__linear_attn_block__geglu_ffn__prompt'

def setup_inputs():

    _w = (lambda a: (lambda t: t.to(torch.bfloat16) if t.is_floating_point() else t)(torch.as_tensor(a)))
    def _A_setup():
        B, S, D = 1, 2048, 576
        return {
            "x": _w((lambda *s: torch.randn(*s, dtype=torch.bfloat16) * 0.05)(B, S, D)),
            "wqkv": _w((lambda *s: torch.randn(*s, dtype=torch.bfloat16) * 0.8)(D, 3 * D)),
            "wo": _w((lambda *s: torch.randn(*s, dtype=torch.bfloat16) * 0.05)(D, D)),
        }
    _A_in = _A_setup()

    def _B_setup():
        B, S, D, F = 1, 2048, 576, 4608
        return {
            "x": _w((lambda *s: torch.randn(*s, dtype=torch.bfloat16) * 0.05)(B, S, D)),
            "w_gate": _w((lambda *s: torch.randn(*s, dtype=torch.bfloat16) * 0.05)(D, F)),
            "w_up": _w((lambda *s: torch.randn(*s, dtype=torch.bfloat16) * 0.05)(D, F)),
            "w_down": _w((lambda *s: torch.randn(*s, dtype=torch.bfloat16) * 0.05)(F, D)),
        }
    _B_in = _B_setup()

    return {
        "A_x": _A_in["x"],
        "A_wqkv": _A_in["wqkv"],
        "A_wo": _A_in["wo"],
        "B_x": _B_in["x"],
        "B_w_gate": _B_in["w_gate"],
        "B_w_up": _B_in["w_up"],
        "B_w_down": _B_in["w_down"],
        "gate_w": _w((lambda *s: torch.randn(*s, dtype=torch.bfloat16) * 0.05)(576) * 16.0),
    }

def reference(A_x, A_wqkv, A_wo, B_x, B_w_gate, B_w_up, B_w_down, gate_w):
    def _A_ref(x, wqkv, wo):
        B, S, D = x.shape
        H = 32; Dh = D // H
        qkv = (lambda x, *d: x.permute(*d))((x @ wqkv).reshape(B, S, 3, H, Dh), 2, 0, 3, 1, 4)
        q, k, v = qkv[0], qkv[1], qkv[2]
        phi = lambda t: torch.where(t > 0, t + 1, torch.exp(t)) + 1e-6
        pq, pk = phi(q), phi(k)
        kv = torch.einsum("bhsd,bhse->bhde", pk, v)
        z = torch.einsum("bhsd->bhd", pk)
        num = torch.einsum("bhqd,bhde->bhqe", pq, kv)
        den = torch.einsum("bhqd,bhd->bhq", pq, z)[..., None] + 1e-6
        o = num / den
        return (lambda x, *d: x.permute(*d))(o, 0, 2, 1, 3).reshape(B, S, D) @ wo

    def _B_ref(x, w_gate, w_up, w_down):
        return (_gelu(x @ w_gate) * (x @ w_up)) @ w_down

    x = A_x
    g = _sigmoid((x * gate_w).sum(-1, keepdims=True))
    a = _A_ref(x, A_wqkv, A_wo)
    b = _B_ref(x, B_w_gate, B_w_up, B_w_down)
    return g * a + (1 - g) * b
```

```cpp
#include <hip/hip_runtime.h>
#include <hip/hip_bf16.h>
typedef __hip_bfloat16 bf16;
static __device__ __forceinline__ float b2f(bf16 v){ return __bfloat162float(v);}
__global__ void gemm_naive(const bf16* A, const bf16* B, float* C, int M, int N, int K){
  __shared__ float sA[32][33], sB[32][33];
  int tx=threadIdx.x, ty=threadIdx.y; int row=blockIdx.y*32+ty, col=blockIdx.x*32+tx;
  float acc=0;
  for(int k0=0;k0<K;k0+=32){
    sA[ty][tx]=b2f(A[(long)row*K+k0+tx]);
    sB[ty][tx]=b2f(B[(long)(k0+ty)*N+col]);
    __syncthreads();
    for(int k=0;k<32;++k) acc+=sA[ty][k]*sB[k][tx];
    __syncthreads();
  }
  C[(long)row*N+col]=acc;
}
__global__ void k_h(const float* G,const float* U,bf16* H,long n){
  long i=blockIdx.x*256L+threadIdx.x; if(i>=n) return;
  float g=G[i],u=U[i];
  float t=0.7978845608028654f*(g+0.044715f*g*g*g);
  float ge=0.5f*g*(1.f+tanhf(t));
  H[i]=__float2bfloat16(ge*u);
}
__device__ float phi(float t){ return (t>0? t+1.f: expf(t))+1e-6f; }
__global__ void k_kv(const float* qkv,float* kv){
  int h=blockIdx.x; int i=threadIdx.x; if(i>=18*19) return; int d=i/19,e=i%19;
  float acc=0;
  for(int s=0;s<2048;++s){ float pk=phi(qkv[(long)s*1728+576+h*18+d]); float v= e<18? qkv[(long)s*1728+1152+h*18+e]:1.f; acc+=pk*v; }
  kv[h*342+i]=acc;
}
__global__ void k_o(const float* qkv,const float* kv,bf16* o){
  int s=blockIdx.x; int c=threadIdx.x; int h=c/18,e=c%18;
  float num=0,den=0;
  for(int d=0;d<18;++d){ float pq=phi(qkv[(long)s*1728+h*18+d]); num+=pq*kv[h*342+d*19+e]; den+=pq*kv[h*342+d*19+18]; }
  o[(long)s*576+c]=__float2bfloat16(num/(den+1e-6f));
}
__global__ void k_fin(const bf16* x,const bf16* gw,const float* a,const float* b,bf16* out){
  __shared__ float red[576];
  int s=blockIdx.x,c=threadIdx.x;
  red[c]=b2f(x[(long)s*576+c])*b2f(gw[c]); __syncthreads();
  float t=0; for(int i=0;i<576;++i) t+=red[i];
  float g=1.f/(1.f+expf(-t));
  out[(long)s*576+c]=__float2bfloat16(g*a[(long)s*576+c]+(1.f-g)*b[(long)s*576+c]);
}
extern "C" void kernel_launch(void* const* d_in, const int* in_sizes, int n_in, void* d_out, int out_size, void* d_ws, size_t ws_size, hipStream_t stream) {
  const bf16* x=(const bf16*)d_in[0]; const bf16* wqkv=(const bf16*)d_in[1]; const bf16* wo=(const bf16*)d_in[2];
  const bf16* wg=(const bf16*)d_in[4]; const bf16* wu=(const bf16*)d_in[5]; const bf16* wd=(const bf16*)d_in[6]; const bf16* gw=(const bf16*)d_in[7];
  char* ws=(char*)d_ws;
  float* qkv=(float*)ws; ws+=2048L*1728*4;
  float* G=(float*)ws; ws+=2048L*4608*4;
  float* U=(float*)ws; ws+=2048L*4608*4;
  bf16* H=(bf16*)ws; ws+=2048L*4608*2;
  float* kv=(float*)ws; ws+=32*342*4+256;
  bf16* o=(bf16*)ws; ws+=2048L*576*2;
  float* a=(float*)ws; ws+=2048L*576*4;
  float* b=(float*)ws; ws+=2048L*576*4;
  dim3 blk(32,32);
  gemm_naive<<<dim3(1728/32,64),blk,0,stream>>>(x,wqkv,qkv,2048,1728,576);
  gemm_naive<<<dim3(4608/32,64),blk,0,stream>>>(x,wg,G,2048,4608,576);
  gemm_naive<<<dim3(4608/32,64),blk,0,stream>>>(x,wu,U,2048,4608,576);
  k_h<<<(2048L*4608+255)/256,256,0,stream>>>(G,U,H,2048L*4608);
  k_kv<<<32,384,0,stream>>>(qkv,kv);
  k_o<<<2048,576,0,stream>>>(qkv,kv,o);
  gemm_naive<<<dim3(576/32,64),blk,0,stream>>>(o,wo,a,2048,576,576);
  gemm_naive<<<dim3(576/32,64),blk,0,stream>>>(H,wd,b,2048,576,4608);
  k_fin<<<2048,576,0,stream>>>(x,gw,a,b,(bf16*)d_out);
}
```

```cpp
#include <hip/hip_runtime.h>
#include <hip/hip_bf16.h>
#include <utility>

typedef __bf16 bf16x8 __attribute__((ext_vector_type(8)));
typedef __bf16 bf16x4 __attribute__((ext_vector_type(4)));
typedef __bf16 bf16x2 __attribute__((ext_vector_type(2)));
typedef float f32x4 __attribute__((ext_vector_type(4)));
typedef float f32x2 __attribute__((ext_vector_type(2)));
typedef unsigned u32x4 __attribute__((ext_vector_type(4)));
typedef unsigned u32x2 __attribute__((ext_vector_type(2)));
#define LAS __attribute__((address_space(3)))
#define DI __device__ __forceinline__
#define RSRC3 0x00020000

constexpr size_t WS_H = 0;
constexpr size_t H_BYTES = (size_t)128 * 144 * 1024;
constexpr size_t WS_WP = WS_H + H_BYTES;
constexpr size_t WP_BYTES = (size_t)4 * 4 * 44 * 9 * 1024;
constexpr size_t WS_PQ = WS_WP + WP_BYTES;
constexpr size_t PQ_BYTES = (size_t)2048 * 1152;
constexpr size_t WS_KV = WS_PQ + PQ_BYTES + 256;
constexpr size_t KV_HEAD = 19 * 24 * 4;
constexpr size_t KV_BYTES = (size_t)16 * 32 * KV_HEAD;
constexpr size_t WS_G = WS_KV + KV_BYTES;
constexpr size_t G_BYTES = 2048 * 4;
constexpr size_t WS_FLAG = WS_G + G_BYTES;
constexpr size_t FLAG_BYTES = (64 + 2048) * 64;
constexpr size_t WS_KVF = WS_FLAG + FLAG_BYTES;
constexpr size_t KVF_BYTES = 64 * 1024;
constexpr size_t WS_X = WS_KVF + KVF_BYTES;
constexpr size_t X_BYTES = (size_t)64 * 8 * 4 * 9 * 1024;
constexpr int K1_LDS = 81920, K2_LDS = 163840;

#define PRIO_MODE 1
#define K2_XROT 0
#define AUX_PRIO 3
#define K2_TOUCH 0
#define H_AUX 16

#if defined(__HIP_DEVICE_COMPILE__)
typedef __amdgpu_buffer_rsrc_t rsrc_t;
constexpr unsigned OOB = 0x7ff00000u;
DI rsrc_t mk_rsrc(const void* p, unsigned bytes) { return __builtin_amdgcn_make_buffer_rsrc((void*)p, (short)0, (int)bytes, RSRC3); }
DI unsigned rfl(unsigned x) { return __builtin_amdgcn_readfirstlane(x); }
template <int N> DI void wait_vm() { static_assert(N >= 0 && N < 64, "vmcnt"); asm volatile("s_waitcnt vmcnt(%0)" ::"i"(N) : "memory"); }
DI void bar() { __builtin_amdgcn_s_barrier(); }
#define SB() __builtin_amdgcn_sched_barrier(0)
DI void dma16(unsigned m0v, unsigned voff, rsrc_t rs, unsigned soff) {
  asm volatile("s_mov_b32 m0, %0\n\ts_nop 0\n\tbuffer_load_dwordx4 %1, %2, %3 offen lds" ::"s"(m0v), "v"(voff), "s"(rs), "s"(soff) : "memory");
}
DI void ld16(u32x4& dst, unsigned voff, rsrc_t rs, unsigned soff) {
  asm volatile("buffer_load_dwordx4 %0, %1, %2, %3 offen" : "=v"(dst) : "v"(voff), "s"(rs), "s"(soff) : "memory");
}
DI void ld16_sc1(u32x4& dst, unsigned voff, rsrc_t rs, unsigned soff) {
  asm volatile("buffer_load_dwordx4 %0, %1, %2, %3 offen sc1" : "=v"(dst) : "v"(voff), "s"(rs), "s"(soff) : "memory");
}
DI void ld4(unsigned& dst, unsigned voff, rsrc_t rs, unsigned soff) {
  asm volatile("buffer_load_dword %0, %1, %2, %3 offen" : "=v"(dst) : "v"(voff), "s"(rs), "s"(soff) : "memory");
}
DI void ld4_sc1(unsigned& dst, unsigned voff, rsrc_t rs, unsigned soff) {
  asm volatile("buffer_load_dword %0, %1, %2, %3 offen sc1" : "=v"(dst) : "v"(voff), "s"(rs), "s"(soff) : "memory");
}
DI void pin(u32x4& a) { asm volatile("" : "+v"(a)); }
DI void pin(unsigned& a) { asm volatile("" : "+v"(a)); }
DI bf16x4 trd(LAS char* p) { return __builtin_amdgcn_ds_read_tr16_b64_v4bf16((LAS bf16x4*)p); }
DI bf16x8 cat(bf16x4 lo, bf16x4 hi) { return __builtin_shufflevector(lo, hi, 0, 1, 2, 3, 4, 5, 6, 7); }
DI f32x4 mfma(bf16x8 a, bf16x8 b, f32x4 c) { return __builtin_amdgcn_mfma_f32_16x16x32_bf16(a, b, c, 0, 0, 0); }
DI void mfma_acc(f32x4& c, bf16x8 a, u32x4 b) { asm volatile("v_mfma_f32_16x16x32_bf16 %0, %1, %2, %0" : "+v"(c) : "v"(a), "v"(b)); }
DI void mfma_zero(f32x4& c, bf16x8 a, u32x4 b) { asm volatile("v_mfma_f32_16x16x32_bf16 %0, %1, %2, 0" : "=&v"(c) : "v"(a), "v"(b)); }
DI void mfma_fence() { SB(); asm volatile("s_nop 7\n\ts_nop 1" ::: "memory"); SB(); }
DI unsigned pk2(float lo, float hi) { f32x2 v = {lo, hi}; return __builtin_bit_cast(unsigned, __builtin_convertvector(v, bf16x2)); }
DI u32x4 gelu_mul8(f32x4 ga, f32x4 gb, f32x4 ua, f32x4 ub) {
  const float c1 = -2.0f * 1.4426950408889634f * 0.7978845608028654f, c2 = c1 * 0.044715f;
  float t0, t1, t2, t3, t4, t5, t6, t7;
#define R8(X) X(8, 0, 16) X(9, 1, 17) X(10, 2, 18) X(11, 3, 19) X(12, 4, 20) X(13, 5, 21) X(14, 6, 22) X(15, 7, 23)
#define S1(g, t, u) "v_mul_f32 %" #t ", %" #g ", %" #g "\n\t"
#define S2(g, t, u) "v_fma_f32 %" #t ", %" #t ", %25, %24\n\t"
#define S3(g, t, u) "v_mul_f32 %" #t ", %" #t ", %" #g "\n\t"
#define S4(g, t, u) "v_exp_f32 %" #t ", %" #t "\n\t"
#define S5(g, t, u) "v_mul_f32 %" #t ", %" #t ", %" #u "\n\t"
#define S6(g, t, u) "v_add_f32 %" #t ", 1.0, %" #t "\n\t"
#define S7(g, t, u) "v_rcp_f32 %" #t ", %" #t "\n\t"
  asm volatile("s_nop 7\n\t" R8(S1) R8(S2) R8(S3) R8(S4) R8(S6) R8(S7) R8(S3) R8(S5)
               "v_cvt_pk_bf16_f32 %0, %0, %1\n\tv_cvt_pk_bf16_f32 %1, %2, %3\n\tv_cvt_pk_bf16_f32 %2, %4, %5\n\tv_cvt_pk_bf16_f32 %3, %6, %7"
               : "=&v"(t0), "=&v"(t1), "=&v"(t2), "=&v"(t3), "=&v"(t4), "=&v"(t5), "=&v"(t6), "=&v"(t7)
               : "v"(ga[0]), "v"(ga[1]), "v"(ga[2]), "v"(ga[3]), "v"(gb[0]), "v"(gb[1]), "v"(gb[2]), "v"(gb[3]),
                 "v"(ua[0]), "v"(ua[1]), "v"(ua[2]), "v"(ua[3]), "v"(ub[0]), "v"(ub[1]), "v"(ub[2]), "v"(ub[3]), "v"(c1), "s"(c2));
  u32x4 o = {__builtin_bit_cast(unsigned, t0), __builtin_bit_cast(unsigned, t1), __builtin_bit_cast(unsigned, t2), __builtin_bit_cast(unsigned, t3)};
  return o;
}
DI float phi(float t) { return (t > 0.f ? t + 1.0f : __builtin_amdgcn_exp2f(t * 1.4426950408889634f)) + 1e-6f; }

#define STAMP(i)
#define RSTAMP(i)

constexpr int HSLOT = 8192;
constexpr int NH = 6, PF = 4;
constexpr int XOFF = NH * HSLOT;

struct Ev { int kind, id, piece; };
constexpr int PRO = 18;
template <int U> constexpr Ev ev_at(int N, int I) {
  if (N < 0) {
    constexpr int k[PRO] = {1, 1, 2, 2, 2, 2, 1, 1, 1, 1, 2, 2, 2, 2, 1, 1, 1, 1}, id[PRO] = {0, 0, 0, 0, 0, 0, 1, 1, 2, 2, 1, 1, 1, 1, 3, 3, 4, 4},
                  pc[PRO] = {0, 1, 0, 1, 2, 3, 0, 1, 0, 1, 0, 1, 2, 3, 0, 1, 0, 1};
    return Ev{k[I], id[I], pc[I]};
  }
  if (I >= 2 * U) return Ev{3, 0, 0};
  const int ks = I / U, t = I % U;
  if ((t == U - PF || t == U - PF + 2) && 2 * N + ks + 5 <= 53) return Ev{1, 2 * N + ks + 5, (t - (U - PF)) / 2};
  if (N + 2 <= 8) {
    if (U == 8 && I < 4) return Ev{2, N + 2, I};
    if (U == 5 && (I == 0 || I == 2 || I == 4 || I == 5)) return Ev{2, N + 2, I == 5 ? 3 : I / 2};
  }
  return Ev{0, 0, 0};
}
template <int U> constexpr int npos(int N) { return N < 0 ? PRO : (N % 9 == 8 && U == 8 ? 2 * U + 4 : 2 * U); }
template <int U> constexpr int younger(int kind, int id, int N, int I) {
  int cnt = 0; bool seen = false;
  for (int n = -1; n <= N; ++n)
    for (int i = 0; i < (n == N ? I : npos<U>(n)); ++i) {
      Ev e = ev_at<U>(n, i);
      if (e.kind == kind && e.id == id) { cnt = 0; seen = true; } else if (e.kind != 0) ++cnt;
    }
  return seen ? cnt : -1;
}
template <int U> constexpr int xrd_at(int I) {
  if (U == 8) return I == 9 ? 0 : I == 10 ? 1 : I == 11 ? 2 : I == 13 ? 3 : -1;
  return I >= 6 ? I - 6 : -1;
}

template <int U> struct Eng {
  int stv;
  rsrc_t r0, r1, rx;
  unsigned wvoff, xvoff[2];
  unsigned m0w, m0x, colb;
  LAS char* rb[U == 8 ? 4 : 5];
  LAS char* xr[2];
  u32x4 x[9][4];
  f32x4 acc[2][U];
  bf16x4 wf[PF + 1][2];
};
constexpr unsigned ROWB(int U) { return U == 8 ? 9216u : 3456u; }
constexpr unsigned PASSB(int U) { return U == 8 ? 128u : 1152u; }
template <int U, int N, int I> DI void eng_event(Eng<U>& c) {
  constexpr Ev e = ev_at<U>(N, I);
  if constexpr (e.kind == 1) {
    constexpr int n = e.id >> 1, ks = e.id & 1;
    constexpr unsigned so = (unsigned)((n % 9) * 64 + ks * 8) * ROWB(U) + (unsigned)(n / 9) * PASSB(U) + (U == 5 ? e.piece * 128u : 0u);
    dma16(c.m0w + (unsigned)((e.id % NH) * HSLOT + e.piece * 4096), c.wvoff, e.piece ? c.r1 : c.r0, c.colb + so);
  } else if constexpr (e.kind == 2) {
    dma16(c.m0x + (unsigned)((e.id & 1) * 4096 + e.piece * 1024), c.xvoff[e.piece & 1], c.rx, (unsigned)(e.id * 128 + e.piece * 8 * 1152));
  }
}
template <int U, int N, int I, int HALF> DI bf16x4 eng_rd(Eng<U>& c) {
  constexpr int ks = I / U, tile = I % U;
  if constexpr (U == 8) return trd(c.rb[tile & 3] + ((2 * N + ks) % NH) * HSLOT + (tile >> 2) * 4096 + HALF * 512);
  else return trd(c.rb[tile] + ((2 * N + ks) % NH) * HSLOT + HALF * 512);
}
template <int U, int N, int I, int HALF> DI void eng_pref(Eng<U>& c) {
  constexpr int B = ((N % 9) * 2 * U + I + PF) % (PF + 1);
  if constexpr (I + PF < 2 * U) c.wf[B][HALF] = eng_rd<U, N, I + PF, HALF>(c);
  else if constexpr (N % 9 < 8) c.wf[B][HALF] = eng_rd<U, N + 1, I + PF - 2 * U, HALF>(c);
}
template <int U, int N, int... Is> DI void eng_prime_(Eng<U>& c, std::integer_sequence<int, Is...>) {
  ((c.wf[(Is >> 1) % (PF + 1)][Is & 1] = eng_rd<U, N, (Is >> 1), (Is & 1)>(c)), ...);
}
template <int U, int N> DI void eng_prime(Eng<U>& c) { eng_prime_<U, N>(c, std::make_integer_sequence<int, 2 * PF>{}); SB(); }
template <int U, int M, int R> DI void eng_xrd(Eng<U>& c) {
  c.x[M][R] = *(LAS u32x4*)(c.xr[R & 1] + (M & 1) * 4096 + (R >> 1) * 2048);
}
template <int U, int N, int I> DI void eng_unit(Eng<U>& c) {
  constexpr int C = N % 9, ks = I / U, tile = I % U, B = (C * 2 * U + I) % (PF + 1);
  if constexpr (tile == U - PF && 2 * N + ks + 1 <= 53) {
    wait_vm<younger<U>(1, 2 * N + ks + 1, N, I)>();
    bar();
  }
  if constexpr (N + 1 <= 8 && xrd_at<U>(I) >= 0) {
    constexpr int M = N + 1 <= 8 ? N + 1 : 0, R = xrd_at<U>(I) >= 0 ? xrd_at<U>(I) : 0;
    if constexpr (R == 0) wait_vm<younger<U>(2, M, N, I)>();
    eng_xrd<U, M, R>(c);
    SB();
  }
  bf16x8 w = cat(c.wf[B][0], c.wf[B][1]);
  if constexpr (C == 0 && ks == 0) mfma_zero(c.acc[0][tile], w, c.x[C][0 + ks]); else mfma_acc(c.acc[0][tile], w, c.x[C][0 + ks]);
  SB(); eng_pref<U, N, I, 0>(c); SB();
  if constexpr (C == 0 && ks == 0) mfma_zero(c.acc[1][tile], w, c.x[C][2 + ks]); else mfma_acc(c.acc[1][tile], w, c.x[C][2 + ks]);
  SB(); eng_pref<U, N, I, 1>(c); SB();
  eng_event<U, N, I>(c); SB();
}
template <int U, int N, int... Is> DI void eng_units(Eng<U>& c, std::integer_sequence<int, Is...>) { (eng_unit<U, N, Is>(c), ...); }
template <int U, int N> DI void eng_chunk(Eng<U>& c) {
  if constexpr (U == 8 && N % 3 == 0) __builtin_amdgcn_s_setprio((N % 9) / 3);
  eng_units<U, N>(c, std::make_integer_sequence<int, 2 * U>{});
}
template <int U, int P> DI void eng_pass(Eng<U>& c) {
  STAMP(1 + P * 4);
  eng_chunk<U, 9 * P + 0>(c); eng_chunk<U, 9 * P + 1>(c); eng_chunk<U, 9 * P + 2>(c);
  STAMP(2 + P * 4);
  eng_chunk<U, 9 * P + 3>(c); eng_chunk<U, 9 * P + 4>(c); eng_chunk<U, 9 * P + 5>(c);
  eng_chunk<U, 9 * P + 6>(c); eng_chunk<U, 9 * P + 7>(c); eng_chunk<U, 9 * P + 8>(c);
  if constexpr (U == 8) __builtin_amdgcn_s_setprio(3);
  STAMP(3 + P * 4);
}
template <int U, int... Is> DI void eng_prologue_(Eng<U>& c, std::integer_sequence<int, Is...>) { (eng_event<U, -1, Is>(c), ...); }
template <int U> DI void eng_start(Eng<U>& c, const void* x, LAS char* lds, int mt, int lane, int wid) {
  c.rx = mk_rsrc(x, 2048 * 1152);
  {
    int o = lane >> 3, swz = ((o >> 1) & 1) | ((wid & 1) << 1);
    c.wvoff = (unsigned)(16 * wid + o) * ROWB(U) + (unsigned)(((lane & 7) ^ (swz << 1)) * 16);
    int f = (o >> 1) & 1;
    unsigned rowb = (unsigned)((mt * 128 + wid * 32 + o) * 1152);
    c.xvoff[0] = rowb + (unsigned)(((lane & 7) ^ f) * 16);
    c.xvoff[1] = rowb + (unsigned)(((lane & 7) ^ f ^ 6) * 16);
  }
  c.m0w = (unsigned)(size_t)lds + (unsigned)wid * 1024u;
  c.m0x = (unsigned)(size_t)lds + (unsigned)(XOFF + wid * 8192);
  {
    LAS char* xb = lds + XOFF + wid * 8192;
    int g = lane >> 4, t16 = lane & 15, f = ((t16 >> 1) & 1) | ((t16 & 8) ? 6 : 0);
    c.xr[0] = xb + t16 * 128 + (((2 * g) ^ f) * 16);
    c.xr[1] = xb + t16 * 128 + (((2 * g + 1) ^ f) * 16);
  }
  c.stv = 0;
  STAMP(0); RSTAMP(13);
  eng_prologue_<U>(c, std::make_integer_sequence<int, PRO>{});
  wait_vm<younger<U>(2, 0, 0, 0)>();
  eng_xrd<U, 0, 0>(c); eng_xrd<U, 0, 1>(c); eng_xrd<U, 0, 2>(c); eng_xrd<U, 0, 3>(c);
  bar();
  eng_prime<U, 0>(c);
}

template <int P, int E> DI void ffn_egroup(Eng<8>& c, rsrc_t rh, unsigned hvoff, unsigned hsoff) {
  constexpr int tt = E >> 1, gi = E & 1;
  if constexpr (E == 3 && P < 2) eng_prime<8, 9 * (P + 1)>(c);
  u32x4 o = gelu_mul8(c.acc[tt][2 * gi], c.acc[tt][2 * gi + 1], c.acc[tt][4 + 2 * gi], c.acc[tt][4 + 2 * gi + 1]);
  __builtin_amdgcn_raw_buffer_store_b128(o, rh, hvoff, hsoff + (unsigned)(P * 2048 + tt * 144 * 1024 + gi * 1024), H_AUX);
  SB();
}
template <int P> DI void ffn_pass(Eng<8>& c, rsrc_t rh, unsigned hvoff, unsigned hsoff) {
  eng_pass<8, P>(c);
  ffn_egroup<P, 0>(c, rh, hvoff, hsoff); ffn_egroup<P, 1>(c, rh, hvoff, hsoff);
  ffn_egroup<P, 2>(c, rh, hvoff, hsoff); ffn_egroup<P, 3>(c, rh, hvoff, hsoff);
  STAMP(4 + P * 4);
}
DI void k1_ffn(const void* x, const void* wg, const void* wu, char* ws, LAS char* lds, int xcd, int idx) {
  const int lane = threadIdx.x & 63, wid = rfl(threadIdx.x >> 6);
  const int s = xcd >> 1, hm = xcd & 1, jj = idx % 6, ml = idx / 6, mt = hm * 8 + ml, j = 6 * s + jj;
  Eng<8> c;
  c.r0 = mk_rsrc(wg, 576 * 9216); c.r1 = mk_rsrc(wu, 576 * 9216);
  rsrc_t rh = mk_rsrc(ws + WS_H, (unsigned)H_BYTES);
  const unsigned hvoff = (unsigned)lane * 16u, hsoff = (unsigned)(((mt * 8 + wid * 2) * 144 + j * 6) * 1024);
  c.colb = (unsigned)j * 384u;
  {
    int g = lane >> 4, q = (lane >> 2) & 3, p = lane & 3, swz = (q >> 1) | ((g & 1) << 1);
#pragma unroll
    for (int i = 0; i < 4; ++i) { c.rb[i] = lds + 1024 * g + 128 * q + 8 * p + ((i ^ swz) * 32); asm volatile("" : "+v"(c.rb[i])); }
  }
  eng_start<8>(c, x, lds, mt, lane, wid);
  ffn_pass<0>(c, rh, hvoff, hsoff); ffn_pass<1>(c, rh, hvoff, hsoff); ffn_pass<2>(c, rh, hvoff, hsoff);
}

constexpr int KVP = 144;
DI void relayout(int task, const void* wo, const void* wd, char* ws, LAS char* lds, int lane, int wid) {
  const bool att = task >= 144;
  const int ta = task - 144, hq = ta / 3, a = ta - 3 * hq;
  rsrc_t rs = att ? mk_rsrc(wo, 576 * 1152) : mk_rsrc(wd, 4608 * 1152);
  rsrc_t rw = mk_rsrc(ws + WS_WP, (unsigned)WP_BYTES);
  const unsigned ldsb = (unsigned)(size_t)lds;
#pragma unroll
  for (int i = 0; i < 9; ++i) {
    const int pi = wid * 9 + i;
    const unsigned b = (unsigned)(pi * 1024 + lane * 16), r = b / 1152u, col = b - r * 1152u;
    const unsigned g = r >> 3, jx = r & 7;
    unsigned src;
    if (!att) src = (unsigned)task * 32u + 16u * (jx >> 2) + 4u * g + (jx & 3);
    else if (a < 2) src = 18u * (unsigned)(4 * hq + 2 * a + (int)(jx >> 2)) + 4u * g + (jx & 3);
    else src = 18u * (unsigned)(4 * hq + (int)g) + 16u + (jx & 3);
    dma16(ldsb + (unsigned)pi * 1024u, src * 1152u + col, rs, 0u);
  }
  wait_vm<0>();
  bar();
  int sidx, ksi;
  if (!att) { sidx = task / 36; ksi = task - 36 * sidx; } else { sidx = hq >> 1; ksi = a < 2 ? 36 + 2 * (hq & 1) + a : 40 + 2 * (hq & 1); }
  const int g = lane >> 4, q = (lane >> 2) & 3, p = lane & 3;
  LAS char* base = lds + (8 * g + q) * 1152 + wid * 288;
#pragma unroll
  for (int u = 0; u < 9; ++u) {
    const int nb = u < 8 ? 64 * (u >> 1) + 16 * p + 8 * (u & 1) : 256 + 8 * p;
    bf16x8 f = cat(trd(base + nb), trd(base + nb + 4 * 1152));
    __builtin_amdgcn_raw_buffer_store_b128(__builtin_bit_cast(u32x4, f), rw, (unsigned)lane * 16u,
                                           (unsigned)((((wid * 4 + sidx) * 44 + ksi) * 9 + u) * 1024), 0);
  }
  asm volatile("s_waitcnt lgkmcnt(0)" ::: "memory");
  bar();
}
template <int P> DI void aux_epi(Eng<5>& c, rsrc_t rpq, unsigned pqoff, LAS char* img, int quad) {
  mfma_fence();
  if constexpr (P < 2) eng_prime<5, 9 * (P + 1)>(c);
#pragma unroll
  for (int tt = 0; tt < 2; ++tt)
#pragma unroll
    for (int t = 0; t < 5; ++t) {
      f32x4 v = c.acc[tt][t];
      u32x2 o;
      if constexpr (P < 2) { o[0] = pk2(phi(v[0]), phi(v[1])); o[1] = pk2(phi(v[2]), phi(v[3])); }
      else { o[0] = pk2(v[0], v[1]); o[1] = pk2(v[2], v[3]); }
      const bool ok = t < 4 || quad < 2;
      if constexpr (P == 0) __builtin_amdgcn_raw_buffer_store_b64(o, rpq, ok ? pqoff : OOB, (unsigned)(tt * 16 * 1152 + t * 32), 0);
      else if (ok) *(LAS u32x2*)(img + tt * 16 * KVP + t * 32) = o;
    }
  mfma_fence();
}
DI void k1_aux(const void* x, const void* wqkv, const void* wo, const void* wd, const void* gw, char* ws, LAS char* lds, int xcd, int a) {
  const int lane = threadIdx.x & 63, wid = rfl(threadIdx.x >> 6);
  const int s = xcd >> 1, hm = xcd & 1, mt = hm * 8 + (a & 7), hg = 2 * s + (a >> 3), auxid = xcd * 16 + a;
  const int g = lane >> 4, q = (lane >> 2) & 3, p = lane & 3, t16 = lane & 15;
  Eng<5> c;
  c.r0 = mk_rsrc(wqkv, 576 * 3456); c.r1 = c.r0;
  c.colb = (unsigned)hg * 128u;
  {
    int swz = (q >> 1) | ((g & 1) << 1);
#pragma unroll
    for (int t = 0; t < 5; ++t) {
      int b = 16 * hg + 32 * t + 8 * p; b = b > 248 ? 248 : b;
      c.rb[t] = lds + (b >> 7) * 4096 + 1024 * g + 128 * q + ((((b >> 5) & 3) ^ swz) * 32) + (b & 31);
      asm volatile("" : "+v"(c.rb[t]));
    }
  }
  if (auxid < 33) {
    u32x4 z = {0u, 0u, 0u, 0u};
    *(u32x4*)(ws + WS_FLAG + (size_t)auxid * 4096 + threadIdx.x * 16) = z;
  }
  rsrc_t rpq = mk_rsrc(ws + WS_PQ, (unsigned)PQ_BYTES);
  const unsigned pqoff = (unsigned)((mt * 128 + wid * 32 + t16) * 1152 + hg * 144 + g * 8);
  LAS char* imgK = lds + XOFF;
  LAS char* imgV = lds;
  const int imgo = (wid * 32 + t16) * KVP + g * 8;
  __builtin_amdgcn_s_setprio(AUX_PRIO);
  eng_start<5>(c, x, lds, mt, lane, wid);
  eng_pass<5, 0>(c); aux_epi<0>(c, rpq, pqoff, nullptr, g); STAMP(4);
  eng_pass<5, 1>(c); aux_epi<1>(c, rpq, pqoff, imgK + imgo, g); STAMP(8);
  eng_pass<5, 2>(c);
  asm volatile("s_waitcnt lgkmcnt(0)" ::: "memory");
  bar();
  aux_epi<2>(c, rpq, pqoff, imgV + imgo, g);
  asm volatile("s_waitcnt vmcnt(0) lgkmcnt(0)" ::: "memory");
  bar();
  {
    const int h = 4 * hg + wid, c0 = (18 * wid) & ~3, off = (18 * wid) & 3;
    f32x4 kv[2][2] = {}, z[2] = {};
    const u32x4 one4 = {0x3f803f80u, 0x3f803f80u, 0x3f803f80u, 0x3f803f80u};
    const bf16x8 ones = __builtin_bit_cast(bf16x8, one4);
    const int ro = (8 * g + q) * KVP + c0 * 2 + p * 8;
#pragma unroll
    for (int ks = 0; ks < 4; ++ks) {
      bf16x8 a[2], b[2];
#pragma unroll
      for (int i = 0; i < 2; ++i) {
        a[i] = cat(trd(imgK + ro + ks * 32 * KVP + i * 32), trd(imgK + ro + ks * 32 * KVP + i * 32 + 4 * KVP));
        b[i] = cat(trd(imgV + ro + ks * 32 * KVP + i * 32), trd(imgV + ro + ks * 32 * KVP + i * 32 + 4 * KVP));
      }
#pragma unroll
      for (int i = 0; i < 2; ++i) {
        kv[i][0] = mfma(a[i], b[0], kv[i][0]); kv[i][1] = mfma(a[i], b[1], kv[i][1]); z[i] = mfma(a[i], ones, z[i]);
      }
    }
    rsrc_t rkv = mk_rsrc(ws + WS_KV, (unsigned)KV_BYTES);
    const unsigned so = (unsigned)((mt * 32 + h) * (int)KV_HEAD);
#pragma unroll
    for (int i = 0; i < 2; ++i) {
      const bool dok = i == 0 || g == 0;
#pragma unroll
      for (int jb = 0; jb < 2; ++jb) {
        const int e = 16 * jb + t16 - off;
        const bool ok = dok && e >= 0 && e < 18;
        __builtin_amdgcn_raw_buffer_store_b128(__builtin_bit_cast(u32x4, kv[i][jb]), rkv, ok ? (unsigned)((e * 24 + 16 * i + 4 * g) * 4) : OOB, so, 0);
      }
      __builtin_amdgcn_raw_buffer_store_b128(__builtin_bit_cast(u32x4, z[i]), rkv, (dok && t16 == 0) ? (unsigned)((18 * 24 + 16 * i + 4 * g) * 4) : OOB, so, 0);
    }
  }
  if (hg == 0) {
    float l0 = 0.f, l1 = 0.f;
#pragma unroll
    for (int cc = 0; cc < 9; ++cc)
#pragma unroll
      for (int ks = 0; ks < 2; ++ks) {
        u32x4 w = *(const u32x4*)((const char*)gw + cc * 128 + g * 32 + ks * 16);
#pragma unroll
        for (int d = 0; d < 4; ++d) {
          const unsigned xa = c.x[cc][ks][d], xb = c.x[cc][2 + ks][d], wv = w[d];
          l0 = __builtin_amdgcn_fdot2_f32_bf16(__builtin_bit_cast(bf16x2, xa), __builtin_bit_cast(bf16x2, wv), l0, false);
          l1 = __builtin_amdgcn_fdot2_f32_bf16(__builtin_bit_cast(bf16x2, xb), __builtin_bit_cast(bf16x2, wv), l1, false);
        }
      }
    l0 += __shfl_xor(l0, 16); l0 += __shfl_xor(l0, 32);
    l1 += __shfl_xor(l1, 16); l1 += __shfl_xor(l1, 32);
    float l = (lane & 16) ? l1 : l0;
    if (lane < 32) ((float*)(ws + WS_G))[mt * 128 + wid * 32 + lane] = 1.0f / (1.0f + __expf(-l));
  }
  asm volatile("s_waitcnt lgkmcnt(0)" ::: "memory");
  bar();
  STAMP(12);
  relayout(auxid, wo, wd, ws, lds, lane, wid);
  if (auxid + 128 < 168) relayout(auxid + 128, wo, wd, ws, lds, lane, wid);
}

constexpr int BLK = 36864, PF2 = 4, JUNK = 4 * BLK;
constexpr int PRO2 = 5 + 4 + 5 + 4 + 2;
constexpr Ev ev2_at(int S, int I) {
  if (S < 0) {
    if (I < 5) return Ev{1, 0, I};
    if (I < 9) return Ev{2, 0, I - 5};
    if (I < 14) return Ev{1, 1, I - 9};
    if (I < 18) return Ev{2, 1, I - 14};
    return Ev{1, 2, I - 18};
  }
  if ((I == 14 || I == 16) && S + 3 <= 10) return Ev{1, S + 3, (I - 14) / 2};
  if ((I == 0 || I == 2 || I == 4) && S + 2 <= 10) return Ev{1, S + 2, 2 + I / 2};
  if ((I == 6 || I == 8 || I == 10 || I == 12) && S + 2 <= 8) return Ev{2, S + 2, (I - 6) / 2};
  if (S == 4 && I == 1) return Ev{4, 0, 0};
  if (S == 6 && (I & 1) && I < 16) return Ev{5, 0, I / 2};
  if (S == 7 && (I & 1) && I < 16) return Ev{6, 0, I / 2};
  if (S == 7 && (I == 6 || I == 8)) return Ev{7, 0, (I - 6) / 2};
  return Ev{0, 0, 0};
}
constexpr int npos2(int S) { return S < 0 ? PRO2 : (S == 10 ? 9 : 18); }
constexpr int younger2(int kind, int id, int S, int I) {
  int cnt = 0; bool seen = false;
  for (int n = -1; n <= S; ++n)
    for (int i = 0; i < (n == S ? I : npos2(n)); ++i) {
      Ev e = ev2_at(n, i);
      if (e.kind == kind && e.id == id) { cnt = 0; seen = true; } else if (e.kind != 0) ++cnt;
    }
  return seen ? cnt : -1;
}
struct G2 {
  int stv;
  rsrc_t rw, rh, rk, rf, rpq, rg;
  unsigned l16, m0w, wsoff, hsoff, ldsb, koff, foff, pqoff[4], goff;
  int wid;
  LAS char* rb[3];
  u32x4 h[3][4];
  u32x4 ob[2][3];
  u32x4 ka[8], pq[8];
  unsigned fl, gt[2], junk0, junk1;
  f32x4 acc[2][9];
  bf16x8 wf[PF2 + 1];
};
template <int S, int I> DI void g2_event(G2& c) {
  constexpr Ev e = ev2_at(S, I);
  if constexpr (e.kind == 1) {
    const int pi = c.wid + 8 * e.piece;
    bool ok = pi < 36;
    if constexpr (e.id == 10) ok = ok && (pi < 9 || (pi >= 18 && pi < 27));
    const unsigned m0v = (e.piece == 4 && pi >= 36) ? c.ldsb + JUNK : c.m0w + (unsigned)((e.id % 4) * BLK + e.piece * 8192);
    dma16(m0v, c.l16, c.rw, ok ? c.wsoff + (unsigned)(e.id * BLK + e.piece * 8192) : OOB);
  } else if constexpr (e.kind == 2) {
    ld16(c.h[e.id % 3][e.piece], c.l16, c.rh, c.hsoff + (unsigned)((e.id * 4 + (e.piece >> 1)) * 1024 + (e.piece & 1) * 144 * 1024));
  } else if constexpr (e.kind == 4) {
    ld4_sc1(c.fl, c.foff, c.rf, 0u);
  } else if constexpr (e.kind == 5) {
    ld16_sc1(c.ka[e.piece], c.l16, c.rk, c.koff + (unsigned)(e.piece * 1024));
  } else if constexpr (e.kind == 6) {
    ld16(c.pq[e.piece], c.pqoff[e.piece >> 1], c.rpq, (unsigned)((e.piece & 1) * 16 * 1152));
  } else if constexpr (e.kind == 7) {
    ld4(c.gt[e.piece], c.goff, c.rg, (unsigned)(e.piece * 64));
  }
}
template <int S, int I> DI bf16x8 g2_rd(G2& c) {
  constexpr int o = (S % 4) * BLK + I * 1024;
  return *(LAS bf16x8*)(c.rb[o / 55296] + o % 55296);
}
template <int S, int I> DI void g2_pref(G2& c) {
  constexpr int B = (S * 18 + I + PF2) % (PF2 + 1);
  if constexpr (I + PF2 < npos2(S)) c.wf[B] = g2_rd<S, I + PF2>(c);
  else if constexpr (S < 10) c.wf[B] = g2_rd<S + 1, I + PF2 - 18>(c);
}
DI void g2_attn(G2& c);
template <int S, int I> DI void g2_unit(G2& c) {
  constexpr int ks = I / 9, u = I % 9, B = (S * 18 + I) % (PF2 + 1);
  if constexpr (I == 0 && S >= 1 && S <= 8) {
    wait_vm<younger2(2, S, S, I)>();
    pin(c.h[S % 3][0]); pin(c.h[S % 3][1]); pin(c.h[S % 3][2]); pin(c.h[S % 3][3]);
  }
  if constexpr (S == 6 && I == 0) {
    static_assert(younger2(4, 0, S, I) >= younger2(2, S, S, I), "flag load covered");
    pin(c.fl);
    unsigned spins = 0;
    while (__builtin_amdgcn_ballot_w64(c.fl != 1u) != 0ull && ++spins < (1u << 20)) {
      __builtin_amdgcn_s_sleep(2);
      asm volatile("buffer_load_dword %0, %1, %2, 0 offen sc1\n\ts_waitcnt vmcnt(0)" : "=&v"(c.fl) : "v"(c.foff), "s"(c.rf) : "memory");
    }
  }
  if constexpr (S == 9 && I == 0) g2_attn(c);
  if constexpr (I == 18 - PF2 && S < 10) { wait_vm<younger2(1, S + 1, S, I)>(); bar(); }
  bf16x8 w = c.wf[B];
  const u32x4 b0 = S <= 8 ? c.h[S % 3][2 * ks] : c.ob[0][S == 9 ? ks : 2], b1 = S <= 8 ? c.h[S % 3][2 * ks + 1] : c.ob[1][S == 9 ? ks : 2];
  if constexpr (S == 0 && ks == 0) mfma_zero(c.acc[0][u], w, b0); else mfma_acc(c.acc[0][u], w, b0);
  SB(); g2_pref<S, I>(c); SB();
  if constexpr (S == 0 && ks == 0) mfma_zero(c.acc[1][u], w, b1); else mfma_acc(c.acc[1][u], w, b1);
  SB(); g2_event<S, I>(c); SB();
}
template <int S, int... Is> DI void g2_units(G2& c, std::integer_sequence<int, Is...>) { (g2_unit<S, Is>(c), ...); }
template <int S> DI void g2_step(G2& c) { g2_units<S>(c, std::make_integer_sequence<int, npos2(S)>{}); }
template <int... Is> DI void g2_prologue(G2& c, std::integer_sequence<int, Is...>) { (g2_event<-1, Is>(c), ...); }
DI void g2_attn(G2& c) {
  wait_vm<younger2(6, 0, 9, 0)>();
#pragma unroll
  for (int i = 0; i < 8; ++i) { pin(c.ka[i]); pin(c.pq[i]); }
  pin(c.gt[0]); pin(c.gt[1]);
  mfma_fence();
#pragma unroll
  for (int tt = 0; tt < 2; ++tt) {
    const float g = __builtin_bit_cast(float, c.gt[tt]), og = 1.0f - g;
#pragma unroll
    for (int u = 0; u < 9; ++u) c.acc[tt][u] *= og;
    float l0 = 0.f, l1 = 0.f;
#pragma unroll
    for (int hh = 0; hh < 4; ++hh) {
      const f32x4 z = {0.f, 0.f, 0.f, 0.f};
      const bf16x8 b = __builtin_bit_cast(bf16x8, c.pq[2 * hh + tt]);
      f32x4 n1 = mfma(__builtin_bit_cast(bf16x8, c.ka[2 * hh]), b, z), n2 = mfma(__builtin_bit_cast(bf16x8, c.ka[2 * hh + 1]), b, z);
      const float sc = g / (n2[2] + 1e-6f);
      c.ob[tt][hh >> 1][2 * (hh & 1)] = pk2(n1[0] * sc, n1[1] * sc);
      c.ob[tt][hh >> 1][2 * (hh & 1) + 1] = pk2(n1[2] * sc, n1[3] * sc);
      l0 += n2[0] * sc; l1 += n2[1] * sc;
    }
    c.ob[tt][2] = u32x4{pk2(l0, l1), 0u, 0u, 0u};
  }
  mfma_fence();
}
DI void kv_reduce(char* ws, int hp, int lane) {
  const int h = hp >> 1, ph = hp & 1, rho = lane & 15, qq = lane >> 4, off = (18 * h) & 3;
  int e = rho;
  if (ph) e = (rho & 3) == 2 ? 18 : (((rho >> 2) == (h & 3) && (rho & 3) < 2) ? 16 + (rho & 3) : -1);
  const bool ok = e >= 0 && qq < 3;
  rsrc_t rkv = mk_rsrc(ws + WS_KV, (unsigned)KV_BYTES);
  const unsigned vo = ok ? (unsigned)(h * (int)KV_HEAD + (e * 24 + 8 * qq) * 4) : OOB;
  const unsigned vo2 = (ok && qq < 2) ? vo + 16u : OOB;
  f32x4 a = {0.f, 0.f, 0.f, 0.f}, b = {0.f, 0.f, 0.f, 0.f};
#pragma unroll
  for (int m = 0; m < 16; ++m) {
    a += __builtin_bit_cast(f32x4, __builtin_amdgcn_raw_buffer_load_b128(rkv, vo, (unsigned)(m * 32 * (int)KV_HEAD), 0));
    b += __builtin_bit_cast(f32x4, __builtin_amdgcn_raw_buffer_load_b128(rkv, vo2, (unsigned)(m * 32 * (int)KV_HEAD), 0));
  }
  float v[8] = {a[0], a[1], a[2], a[3], b[0], b[1], b[2], b[3]};
#pragma unroll
  for (int j = 0; j < 8; ++j) { const int dl = 8 * qq + j - off; v[j] = (dl >= 0 && dl < 18) ? v[j] : 0.f; }
  u32x4 o = {pk2(v[0], v[1]), pk2(v[2], v[3]), pk2(v[4], v[5]), pk2(v[6], v[7])};
  rsrc_t rk = mk_rsrc(ws + WS_KVF, (unsigned)KVF_BYTES);
  __builtin_amdgcn_raw_buffer_store_b128(o, rk, (unsigned)lane * 16u, (unsigned)hp * 1024u, 16);
}
DI void k2_body(char* ws, void* out, LAS char* lds) {
  const int lane = threadIdx.x & 63, wid = rfl(threadIdx.x >> 6);
  const int xcd = (blockIdx.x + K2_XROT) & 7, idx = blockIdx.x >> 3, s = xcd >> 1, hm = xcd & 1, m = hm * 8 + (idx >> 2), ng = idx & 3;
  const int tq = wid & 3, gam = wid >> 2, t16 = lane & 15, quad = lane >> 4, tile = m * 4 + ng;
  G2 c;
  c.wid = wid; c.stv = 0;
  c.rw = mk_rsrc(ws + WS_WP, (unsigned)WP_BYTES); c.rh = mk_rsrc(ws + WS_H, (unsigned)H_BYTES);
  c.rk = mk_rsrc(ws + WS_KVF, (unsigned)KVF_BYTES); c.rf = mk_rsrc(ws + WS_FLAG, (unsigned)FLAG_BYTES);
  c.rpq = mk_rsrc(ws + WS_PQ, (unsigned)PQ_BYTES); c.rg = mk_rsrc(ws + WS_G, (unsigned)G_BYTES);
  c.l16 = (unsigned)lane * 16u;
  c.ldsb = (unsigned)(size_t)lds;
  c.m0w = c.ldsb + (unsigned)wid * 1024u;
  c.wsoff = (unsigned)((ng * 4 + s) * 44 * 9216 + wid * 1024);
  c.hsoff = (unsigned)(((m * 8 + tq * 2) * 144 + 36 * s + 2 * gam) * 1024);
  const int h0 = 8 * s + 4 * gam;
  c.koff = (unsigned)(h0 * 2048);
  c.foff = (unsigned)((2 * h0 + (lane & 7)) * 64);
#pragma unroll
  for (int hh = 0; hh < 4; ++hh) c.pqoff[hh] = (unsigned)((m * 128 + tq * 32 + t16) * 1152 + (((18 * (h0 + hh)) & ~3) + 8 * quad) * 2);
  c.goff = (unsigned)((m * 128 + tq * 32 + t16) * 4);
#pragma unroll
  for (int i = 0; i < 3; ++i) { c.rb[i] = lds + gam * 18432 + lane * 16 + i * 55296; asm volatile("" : "+v"(c.rb[i])); }
  STAMP(0); RSTAMP(13);
  g2_prologue(c, std::make_integer_sequence<int, PRO2>{});
  const bool red = blockIdx.x < 64 && wid == 7;
  if (red) kv_reduce(ws, blockIdx.x, lane);
  wait_vm<younger2(2, 0, 0, 0)>();
  pin(c.h[0][0]); pin(c.h[0][1]); pin(c.h[0][2]); pin(c.h[0][3]);
  bar();
  c.wf[0] = g2_rd<0, 0>(c); c.wf[1] = g2_rd<0, 1>(c); c.wf[2] = g2_rd<0, 2>(c); c.wf[3] = g2_rd<0, 3>(c);
  SB();
  STAMP(1);
  g2_step<0>(c); g2_step<1>(c);
  if (red) {
    wait_vm<0>();
    if (lane == 0) __hip_atomic_store((unsigned*)(ws + WS_FLAG + blockIdx.x * 64), 1u, __ATOMIC_RELAXED, __HIP_MEMORY_SCOPE_AGENT);
  }
  STAMP(2);
  g2_step<2>(c); g2_step<3>(c); STAMP(9); g2_step<4>(c); g2_step<5>(c); STAMP(10); g2_step<6>(c); STAMP(11); g2_step<7>(c); STAMP(12); g2_step<8>(c);
  STAMP(3);
  g2_step<9>(c); g2_step<10>(c);
  mfma_fence();
  STAMP(4);
  asm volatile("s_waitcnt vmcnt(0) lgkmcnt(0)" ::: "memory");
  bar();
  {
    LAS f32x4* mine = (LAS f32x4*)(lds + (wid * 9) * 1024 + lane * 16);
    LAS f32x4* peer = (LAS f32x4*)(lds + ((wid ^ 4) * 9) * 1024 + lane * 16);
#pragma unroll
    for (int u = 0; u < 9; ++u) mine[u * 64] = gam ? c.acc[0][u] : c.acc[1][u];
    asm volatile("s_waitcnt lgkmcnt(0)" ::: "memory");
    bar();
#pragma unroll
    for (int u = 0; u < 9; ++u) c.acc[0][u] = (gam ? c.acc[1][u] : c.acc[0][u]) + peer[u * 64];
  }
  STAMP(5);
  rsrc_t rxc = mk_rsrc(ws + WS_X, (unsigned)X_BYTES);
  const unsigned xo = (unsigned)((tile * 8 + wid) * 4 * 9216);
  const bool own8 = tq == s;
#pragma unroll
  for (int u = 0; u < 9; ++u) {
    const bool keep = u < 8 ? (u >> 1) == s : own8;
    __builtin_amdgcn_raw_buffer_store_b128(__builtin_bit_cast(u32x4, c.acc[0][u]), rxc, c.l16, keep ? OOB : xo + (unsigned)(s * 9216 + u * 1024), 16);
  }
  f32x4 r0 = c.acc[0][0], r1 = c.acc[0][1], r8 = c.acc[0][8];
#pragma unroll
  for (int P = 1; P < 4; ++P) if (s == P) { r0 = c.acc[0][2 * P]; r1 = c.acc[0][2 * P + 1]; }
  asm volatile("s_waitcnt vmcnt(0)" ::: "memory");
  unsigned* fl_base = (unsigned*)(ws + WS_FLAG + (64 + (tile * 8 + wid) * 4) * 64);
  if (lane == 0) __hip_atomic_store(fl_base + s * 16, 1u, __ATOMIC_RELAXED, __HIP_MEMORY_SCOPE_AGENT);
  STAMP(6);
  {
    const unsigned fo = (unsigned)((64 + (tile * 8 + wid) * 4 + (lane & 3)) * 64);
    unsigned fl, spins = 0;
    do {
      asm volatile("buffer_load_dword %0, %1, %2, 0 offen sc1\n\ts_waitcnt vmcnt(0)" : "=&v"(fl) : "v"(fo), "s"(c.rf) : "memory");
    } while (__builtin_amdgcn_ballot_w64(fl != 1u && (lane & 3) != s) != 0ull && ++spins < (1u << 22));
    STAMP(7);
    u32x4 pv[3][3];
#pragma unroll
    for (int k = 0; k < 3; ++k) {
      const int src = k + (k >= s ? 1 : 0);
      const unsigned so = xo + (unsigned)(src * 9216);
      pv[k][0] = __builtin_amdgcn_raw_buffer_load_b128(rxc, c.l16, so + (unsigned)(s * 2048), 16);
      pv[k][1] = __builtin_amdgcn_raw_buffer_load_b128(rxc, c.l16, so + (unsigned)(s * 2048 + 1024), 16);
      pv[k][2] = __builtin_amdgcn_raw_buffer_load_b128(rxc, c.l16, own8 ? so + 8192u : OOB, 16);
    }
#pragma unroll
    for (int k = 0; k < 3; ++k) {
      r0 += __builtin_bit_cast(f32x4, pv[k][0]); r1 += __builtin_bit_cast(f32x4, pv[k][1]); r8 += __builtin_bit_cast(f32x4, pv[k][2]);
    }
    char* orow = (char*)out + (size_t)(m * 128 + tq * 32 + gam * 16 + t16) * 1152 + ng * 288;
    u32x4 o = {pk2(r0[0], r0[1]), pk2(r0[2], r0[3]), pk2(r1[0], r1[1]), pk2(r1[2], r1[3])};
    *(u32x4*)(orow + s * 64 + quad * 16) = o;
    if (own8) { u32x2 o8 = {pk2(r8[0], r8[1]), pk2(r8[2], r8[3])}; *(u32x2*)(orow + 256 + quad * 8) = o8; }
    STAMP(8);
  }
}
#endif

__global__ __launch_bounds__(256, 2) void k1(const void* x, const void* wqkv, const void* wo, const void* wg, const void* wu,
                                             const void* wd, const void* gw, char* ws) {
#if defined(__HIP_DEVICE_COMPILE__)
  extern __shared__ __attribute__((aligned(16))) char smem[];
  LAS char* lds = (LAS char*)smem;
  const int xcd = blockIdx.x & 7, idx = blockIdx.x >> 3;
  if (idx < 48) k1_ffn(x, wg, wu, ws, lds, xcd, idx);
  else k1_aux(x, wqkv, wo, wd, gw, ws, lds, xcd, idx - 48);
#endif
}
__global__ __launch_bounds__(512, 2) void k2(char* ws, void* out) {
#if defined(__HIP_DEVICE_COMPILE__)
  extern __shared__ __attribute__((aligned(16))) char smem[];
  k2_body(ws, out, (LAS char*)smem);
#endif
}

extern "C" void kernel_launch(void* const* d_in, const int* in_sizes, int n_in, void* d_out, int out_size, void* d_ws, size_t ws_size,
                              hipStream_t stream) {
  (void)hipFuncSetAttribute((const void*)k1, hipFuncAttributeMaxDynamicSharedMemorySize, K1_LDS);
  (void)hipFuncSetAttribute((const void*)k2, hipFuncAttributeMaxDynamicSharedMemorySize, K2_LDS);
  k1<<<512, 256, K1_LDS, stream>>>(d_in[0], d_in[1], d_in[2], d_in[4], d_in[5], d_in[6], d_in[7], (char*)d_ws);
  k2<<<256, 512, K2_LDS, stream>>>((char*)d_ws, d_out);
}
```

```cpp
#include <hip/hip_runtime.h>
#include <hip/hip_bf16.h>
#include <utility>

typedef __bf16 bf16x8 __attribute__((ext_vector_type(8)));
typedef __bf16 bf16x4 __attribute__((ext_vector_type(4)));
typedef __bf16 bf16x2 __attribute__((ext_vector_type(2)));
typedef float f32x4 __attribute__((ext_vector_type(4)));
typedef float f32x2 __attribute__((ext_vector_type(2)));
typedef unsigned u32x4 __attribute__((ext_vector_type(4)));
typedef unsigned u32x2 __attribute__((ext_vector_type(2)));
#define LAS __attribute__((address_space(3)))
#define DI __device__ __forceinline__
#define RSRC3 0x00020000

constexpr size_t WS_H = 0;
constexpr size_t H_BYTES = (size_t)128 * 144 * 1024;
constexpr size_t WS_WP = WS_H + H_BYTES;
constexpr size_t WP_BYTES = (size_t)4 * 4 * 44 * 9 * 1024;
constexpr size_t WS_PQ = WS_WP + WP_BYTES;
constexpr size_t PQ_BYTES = (size_t)128 * 32 * 768;
constexpr size_t WS_KV = WS_PQ + PQ_BYTES;
constexpr size_t KV_HEAD = 19 * 24 * 4;
constexpr size_t KV_BYTES = (size_t)16 * 32 * KV_HEAD;
constexpr size_t WS_G = WS_KV + KV_BYTES;
constexpr size_t G_BYTES = 2048 * 4;
constexpr size_t WS_KVF = WS_G + G_BYTES;
constexpr size_t KVF_BYTES = 64 * 1024;
constexpr size_t WS_X = WS_KVF + KVF_BYTES;
constexpr unsigned XUNIT = 13824;
constexpr size_t X_BYTES = (size_t)512 * XUNIT;
constexpr unsigned EMPTY = 0xffffffffu;
constexpr int K1_LDS = 81920, K2_LDS = 163840;

#define PRIO_MODE 1
#define MAPB 1
#define AUX_PRIO 0
#define S2_SLEEP 70
#define S2_N 1
#define ROT_ON 1
#define CODE_PF 1
#define NT_W 0
#define NT_X 0
#define HELP_PRIO 3
#define H_AUX 16

#if defined(__HIP_DEVICE_COMPILE__)
typedef __amdgpu_buffer_rsrc_t rsrc_t;
constexpr unsigned OOB = 0x7ff00000u;
DI rsrc_t mk_rsrc(const void* p, unsigned bytes) { return __builtin_amdgcn_make_buffer_rsrc((void*)p, (short)0, (int)bytes, RSRC3); }
DI unsigned rfl(unsigned x) { return __builtin_amdgcn_readfirstlane(x); }
template <int N> DI void wait_vm() { static_assert(N >= 0 && N < 64, "vmcnt"); asm volatile("s_waitcnt vmcnt(%0)" ::"i"(N) : "memory"); }
DI void bar() { __builtin_amdgcn_s_barrier(); }
#define SB() __builtin_amdgcn_sched_barrier(0)
DI void dma16(unsigned m0v, unsigned voff, rsrc_t rs, unsigned soff) {
  asm volatile("s_mov_b32 m0, %0\n\ts_nop 0\n\tbuffer_load_dwordx4 %1, %2, %3 offen lds" ::"s"(m0v), "v"(voff), "s"(rs), "s"(soff) : "memory");
}
DI void dma16_nt(unsigned m0v, unsigned voff, rsrc_t rs, unsigned soff) {
  asm volatile("s_mov_b32 m0, %0\n\ts_nop 0\n\tbuffer_load_dwordx4 %1, %2, %3 offen nt lds" ::"s"(m0v), "v"(voff), "s"(rs), "s"(soff) : "memory");
}
DI void ld16(u32x4& dst, unsigned voff, rsrc_t rs, unsigned soff) {
  asm volatile("buffer_load_dwordx4 %0, %1, %2, %3 offen" : "=v"(dst) : "v"(voff), "s"(rs), "s"(soff) : "memory");
}
DI void ld16_sc1(u32x4& dst, unsigned voff, rsrc_t rs, unsigned soff) {
  asm volatile("buffer_load_dwordx4 %0, %1, %2, %3 offen sc1" : "=v"(dst) : "v"(voff), "s"(rs), "s"(soff) : "memory");
}
DI void ld4(unsigned& dst, unsigned voff, rsrc_t rs, unsigned soff) {
  asm volatile("buffer_load_dword %0, %1, %2, %3 offen" : "=v"(dst) : "v"(voff), "s"(rs), "s"(soff) : "memory");
}
DI void ld4_sc1(unsigned& dst, unsigned voff, rsrc_t rs, unsigned soff) {
  asm volatile("buffer_load_dword %0, %1, %2, %3 offen sc1" : "=v"(dst) : "v"(voff), "s"(rs), "s"(soff) : "memory");
}
DI void touch(unsigned& dst, unsigned voff, rsrc_t rs, unsigned soff) {
  asm volatile("buffer_load_dword %0, %1, %2, %3 offen" : "+v"(dst) : "v"(voff), "s"(rs), "s"(soff) : "memory");
}
DI void dma16_half(unsigned m0v, unsigned voff, rsrc_t rs, unsigned soff) {
  asm volatile("s_mov_b32 m0, %0\n\ts_mov_b32 exec_hi, 0\n\tbuffer_load_dwordx4 %1, %2, %3 offen lds\n\ts_mov_b32 exec_hi, -1"
               ::"s"(m0v), "v"(voff), "s"(rs), "s"(soff) : "memory");
}
DI void pin(u32x4& a) { asm volatile("" : "+v"(a)); }
DI void pin(unsigned& a) { asm volatile("" : "+v"(a)); }
DI bf16x4 trd(LAS char* p) { return __builtin_amdgcn_ds_read_tr16_b64_v4bf16((LAS bf16x4*)p); }
DI bf16x8 cat(bf16x4 lo, bf16x4 hi) { return __builtin_shufflevector(lo, hi, 0, 1, 2, 3, 4, 5, 6, 7); }
DI f32x4 mfma(bf16x8 a, bf16x8 b, f32x4 c) { return __builtin_amdgcn_mfma_f32_16x16x32_bf16(a, b, c, 0, 0, 0); }
DI void mfma_acc(f32x4& c, bf16x8 a, u32x4 b) { asm volatile("v_mfma_f32_16x16x32_bf16 %0, %1, %2, %0" : "+v"(c) : "v"(a), "v"(b)); }
DI void mfma_zero(f32x4& c, bf16x8 a, u32x4 b) { asm volatile("v_mfma_f32_16x16x32_bf16 %0, %1, %2, 0" : "=&v"(c) : "v"(a), "v"(b)); }
DI void mfma_fence() { SB(); asm volatile("s_nop 7\n\ts_nop 1" ::: "memory"); SB(); }
DI unsigned pk2(float lo, float hi) { f32x2 v = {lo, hi}; return __builtin_bit_cast(unsigned, __builtin_convertvector(v, bf16x2)); }
DI u32x4 gelu_mul8(f32x4 ga, f32x4 gb, f32x4 ua, f32x4 ub) {
  const float c1 = -2.0f * 1.4426950408889634f * 0.7978845608028654f, c2 = c1 * 0.044715f;
  float t0, t1, t2, t3, t4, t5, t6, t7;
#define R8(X) X(8, 0, 16) X(9, 1, 17) X(10, 2, 18) X(11, 3, 19) X(12, 4, 20) X(13, 5, 21) X(14, 6, 22) X(15, 7, 23)
#define S1(g, t, u) "v_mul_f32 %" #t ", %" #g ", %" #g "\n\t"
#define S2(g, t, u) "v_fma_f32 %" #t ", %" #t ", %25, %24\n\t"
#define S3(g, t, u) "v_mul_f32 %" #t ", %" #t ", %" #g "\n\t"
#define S4(g, t, u) "v_exp_f32 %" #t ", %" #t "\n\t"
#define S5(g, t, u) "v_mul_f32 %" #t ", %" #t ", %" #u "\n\t"
#define S6(g, t, u) "v_add_f32 %" #t ", 1.0, %" #t "\n\t"
#define S7(g, t, u) "v_rcp_f32 %" #t ", %" #t "\n\t"
  asm volatile("s_nop 7\n\t" R8(S1) R8(S2) R8(S3) R8(S4) R8(S6) R8(S7) R8(S3) R8(S5)
               "v_cvt_pk_bf16_f32 %0, %0, %1\n\tv_cvt_pk_bf16_f32 %1, %2, %3\n\tv_cvt_pk_bf16_f32 %2, %4, %5\n\tv_cvt_pk_bf16_f32 %3, %6, %7"
               : "=&v"(t0), "=&v"(t1), "=&v"(t2), "=&v"(t3), "=&v"(t4), "=&v"(t5), "=&v"(t6), "=&v"(t7)
               : "v"(ga[0]), "v"(ga[1]), "v"(ga[2]), "v"(ga[3]), "v"(gb[0]), "v"(gb[1]), "v"(gb[2]), "v"(gb[3]),
                 "v"(ua[0]), "v"(ua[1]), "v"(ua[2]), "v"(ua[3]), "v"(ub[0]), "v"(ub[1]), "v"(ub[2]), "v"(ub[3]), "v"(c1), "s"(c2));
  u32x4 o = {__builtin_bit_cast(unsigned, t0), __builtin_bit_cast(unsigned, t1), __builtin_bit_cast(unsigned, t2), __builtin_bit_cast(unsigned, t3)};
  return o;
}
DI float phi(float t) { return t > 0.f ? t + 1.000001f : __builtin_amdgcn_exp2f(t * 1.4426950408889634f) + 1e-6f; }
DI void fill_empty(char* ws, int unit, int lane, int wid) {
  rsrc_t rx = mk_rsrc(ws + WS_X, (unsigned)X_BYTES);
  const u32x4 e = {EMPTY, EMPTY, EMPTY, EMPTY};
#pragma unroll
  for (int i = 0; i < 4; ++i) {
    const unsigned o = (unsigned)((wid * 4 + i) * 1024 + lane * 16);
    __builtin_amdgcn_raw_buffer_store_b128(e, rx, o < XUNIT ? o : OOB, (unsigned)unit * XUNIT, 0);
  }
}

#define STAMP(i)
#define RSTAMP(i)

constexpr int HSLOT = 8192;
constexpr int NH = 6, PF = 4;
constexpr int XOFF = NH * HSLOT;

struct Ev { int kind, id, piece; };
constexpr int PRO = 14, NT0 = 0;
template <int U> constexpr Ev ev_at(int N, int I) {
  if (N < 0) {
    if (I >= PRO) return Ev{1, 3 + (I - PRO) / 2, (I - PRO) & 1};
    constexpr int k[PRO] = {1, 1, 2, 2, 2, 2, 1, 1, 1, 1, 2, 2, 2, 2}, id[PRO] = {0, 0, 0, 0, 0, 0, 1, 1, 2, 2, 1, 1, 1, 1},
                  pc[PRO] = {0, 1, 0, 1, 2, 3, 0, 1, 0, 1, 0, 1, 2, 3};
    return Ev{k[I], id[I], pc[I]};
  }
  if (N == 0) {
    if (U == 8) {
      if (I < 4) return Ev{1, 3 + I / 2, I & 1};
      if (I == 5 || I == 7 || I == 8 || I == 10) return Ev{2, 2, I == 5 ? 0 : I == 7 ? 1 : I == 8 ? 2 : 3};
    }
  }
  if (I >= 2 * U) return Ev{3, 0, 0};
  const int ks = I / U, t = I % U;
  if ((t == U - PF || t == U - PF + 2) && 2 * N + ks + 5 <= 53) return Ev{1, 2 * N + ks + 5, (t - (U - PF)) / 2};
  if (N + 2 <= 8) {
    if (U == 8 && I < 4) return Ev{2, N + 2, I};
    if (U == 5 && (I == 0 || I == 2 || I == 4 || I == 5)) return Ev{2, N + 2, I == 5 ? 3 : I / 2};
  }
  return Ev{0, 0, 0};
}
constexpr int eops(int P) { return 4; }
template <int U> constexpr int npos(int N) { return N < 0 ? (U == 8 ? PRO : PRO + 4) : (N % 9 == 8 && U == 8 ? 2 * U + eops(N / 9) : 2 * U); }
template <int U> constexpr int younger(int kind, int id, int N, int I) {
  int cnt = 0; bool seen = false;
  for (int n = -1; n <= N; ++n)
    for (int i = 0; i < (n == N ? I : npos<U>(n)); ++i) {
      Ev e = ev_at<U>(n, i);
      if (e.kind == kind && e.id == id) { cnt = 0; seen = true; } else if (e.kind != 0) ++cnt;
    }
  return seen ? cnt : -1;
}
template <int U> constexpr int xrd_at(int I) {
  if (U == 8) return I == 9 ? 0 : I == 10 ? 1 : I == 11 ? 2 : I == 13 ? 3 : -1;
  return I >= 6 ? I - 6 : -1;
}

template <int U> struct Eng {
  int stv;
  rsrc_t r0, r1, rx;
  unsigned wvoff, xvoff[2];
  unsigned m0w, m0x, colb, dummy;
  int rot;
  LAS char* rb[U == 8 ? 4 : 5];
  LAS char* xr[2];
  u32x4 x[9][4];
  f32x4 acc[2][U];
  bf16x4 wf[PF + 1][2];
};
constexpr unsigned ROWB(int U) { return U == 8 ? 9216u : 3456u; }
constexpr unsigned PASSB(int U) { return U == 8 ? 128u : 1152u; }
DI unsigned kc_of(int rot, int C) { const int kc = C + rot; return (unsigned)(kc >= 9 ? kc - 9 : kc); }
template <int U, int N, int I> DI void eng_event(Eng<U>& c) {
  constexpr Ev e = ev_at<U>(N, I);
  if constexpr (e.kind == 1) {
    constexpr int n = e.id >> 1, ks = e.id & 1;
    constexpr unsigned so = (unsigned)(ks * 8) * ROWB(U) + (unsigned)(n / 9) * PASSB(U) + (U == 5 ? e.piece * 128u : 0u);
    if constexpr ((e.id % 18) < NT_W)
      dma16_nt(c.m0w + (unsigned)((e.id % NH) * HSLOT + e.piece * 4096), c.wvoff, e.piece ? c.r1 : c.r0, kc_of(c.rot, n % 9) * (64u * ROWB(U)) + c.colb + so);
    else
      dma16(c.m0w + (unsigned)((e.id % NH) * HSLOT + e.piece * 4096), c.wvoff, e.piece ? c.r1 : c.r0, kc_of(c.rot, n % 9) * (64u * ROWB(U)) + c.colb + so);
  } else if constexpr (e.kind == 2) {
    if constexpr (e.id < NT_X)
      dma16_nt(c.m0x + (unsigned)((e.id & 1) * 4096 + e.piece * 1024), c.xvoff[e.piece & 1], c.rx, kc_of(c.rot, e.id) * 128u + (unsigned)(e.piece * 8 * 1152));
    else
      dma16(c.m0x + (unsigned)((e.id & 1) * 4096 + e.piece * 1024), c.xvoff[e.piece & 1], c.rx, kc_of(c.rot, e.id) * 128u + (unsigned)(e.piece * 8 * 1152));
  }
}
template <int U, int N, int I, int HALF> DI bf16x4 eng_rd(Eng<U>& c) {
  constexpr int ks = I / U, tile = I % U;
  if constexpr (U == 8) return trd(c.rb[tile & 3] + ((2 * N + ks) % NH) * HSLOT + (tile >> 2) * 4096 + HALF * 512);
  else return trd(c.rb[tile] + ((2 * N + ks) % NH) * HSLOT + HALF * 512);
}
template <int U, int N, int I, int HALF> DI void eng_pref(Eng<U>& c) {
  constexpr int B = ((N % 9) * 2 * U + I + PF) % (PF + 1);
  if constexpr (I + PF < 2 * U) c.wf[B][HALF] = eng_rd<U, N, I + PF, HALF>(c);
  else if constexpr (N % 9 < 8) c.wf[B][HALF] = eng_rd<U, N + 1, I + PF - 2 * U, HALF>(c);
}
template <int U, int N, int... Is> DI void eng_prime_(Eng<U>& c, std::integer_sequence<int, Is...>) {
  ((c.wf[(Is >> 1) % (PF + 1)][Is & 1] = eng_rd<U, N, (Is >> 1), (Is & 1)>(c)), ...);
}
template <int U, int N> DI void eng_prime(Eng<U>& c) { eng_prime_<U, N>(c, std::make_integer_sequence<int, 2 * PF>{}); SB(); }
template <int U, int M, int R> DI void eng_xrd(Eng<U>& c) {
  c.x[M][R] = *(LAS u32x4*)(c.xr[R & 1] + (M & 1) * 4096 + (R >> 1) * 2048);
}
template <int U, int N, int I> DI void eng_unit(Eng<U>& c) {
  constexpr int C = N % 9, ks = I / U, tile = I % U, B = (C * 2 * U + I) % (PF + 1);
  if constexpr (tile == U - PF && 2 * N + ks + 1 <= 53) {
    wait_vm<younger<U>(1, 2 * N + ks + 1, N, I)>();
    bar();
  }
  if constexpr (N + 1 <= 8 && xrd_at<U>(I) >= 0) {
    constexpr int M = N + 1 <= 8 ? N + 1 : 0, R = xrd_at<U>(I) >= 0 ? xrd_at<U>(I) : 0;
    if constexpr (R == 0) wait_vm<younger<U>(2, M, N, I)>();
    eng_xrd<U, M, R>(c);
    SB();
  }
  bf16x8 w = cat(c.wf[B][0], c.wf[B][1]);
  if constexpr (C == 0 && ks == 0) mfma_zero(c.acc[0][tile], w, c.x[C][0 + ks]); else mfma_acc(c.acc[0][tile], w, c.x[C][0 + ks]);
  SB(); eng_pref<U, N, I, 0>(c); SB();
  if constexpr (C == 0 && ks == 0) mfma_zero(c.acc[1][tile], w, c.x[C][2 + ks]); else mfma_acc(c.acc[1][tile], w, c.x[C][2 + ks]);
  SB(); eng_pref<U, N, I, 1>(c); SB();
  eng_event<U, N, I>(c); SB();
}
template <int U, int N, int... Is> DI void eng_units(Eng<U>& c, std::integer_sequence<int, Is...>) { (eng_unit<U, N, Is>(c), ...); }
template <int U, int N> DI void eng_chunk(Eng<U>& c) {
  if constexpr (U == 8 && N % 3 == 0) __builtin_amdgcn_s_setprio((N % 9) / 3);
  eng_units<U, N>(c, std::make_integer_sequence<int, 2 * U>{});
}
template <int U, int P> DI void eng_pass(Eng<U>& c) {
  STAMP(1 + P * 4);
  eng_chunk<U, 9 * P + 0>(c); eng_chunk<U, 9 * P + 1>(c); eng_chunk<U, 9 * P + 2>(c);
  STAMP(2 + P * 4);
  eng_chunk<U, 9 * P + 3>(c); eng_chunk<U, 9 * P + 4>(c); eng_chunk<U, 9 * P + 5>(c);
  eng_chunk<U, 9 * P + 6>(c); eng_chunk<U, 9 * P + 7>(c); eng_chunk<U, 9 * P + 8>(c);
  if constexpr (U == 8) __builtin_amdgcn_s_setprio(3);
  STAMP(3 + P * 4);
}
template <int U, int... Is> DI void eng_prologue_(Eng<U>& c, std::integer_sequence<int, Is...>) { (eng_event<U, -1, Is>(c), ...); }
template <int U> DI void eng_start(Eng<U>& c, const void* x, LAS char* lds, int mt, int lane, int wid) {
  c.rx = mk_rsrc(x, 2048 * 1152);
  {
    int o = lane >> 3, swz = ((o >> 1) & 1) | ((wid & 1) << 1);
    c.wvoff = (unsigned)(16 * wid + o) * ROWB(U) + (unsigned)(((lane & 7) ^ (swz << 1)) * 16);
    int f = (o >> 1) & 1;
    unsigned rowb = (unsigned)((mt * 128 + wid * 32 + o) * 1152);
    c.xvoff[0] = rowb + (unsigned)(((lane & 7) ^ f) * 16);
    c.xvoff[1] = rowb + (unsigned)(((lane & 7) ^ f ^ 6) * 16);
  }
  c.m0w = (unsigned)(size_t)lds + (unsigned)wid * 1024u;
  c.m0x = (unsigned)(size_t)lds + (unsigned)(XOFF + wid * 8192);
  {
    LAS char* xb = lds + XOFF + wid * 8192;
    int g = lane >> 4, t16 = lane & 15, f = ((t16 >> 1) & 1) | ((t16 & 8) ? 6 : 0);
    c.xr[0] = xb + t16 * 128 + (((2 * g) ^ f) * 16);
    c.xr[1] = xb + t16 * 128 + (((2 * g + 1) ^ f) * 16);
  }
  eng_prologue_<U>(c, std::make_integer_sequence<int, npos<U>(-1)>{});
  wait_vm<younger<U>(2, 0, 0, 0)>();
  eng_xrd<U, 0, 0>(c); eng_xrd<U, 0, 1>(c); eng_xrd<U, 0, 2>(c); eng_xrd<U, 0, 3>(c);
  bar();
  eng_prime<U, 0>(c);
}

struct Rl { rsrc_t rs; unsigned r0, rs4, dst; bool on; };
DI Rl rl_task(int task, const void* wo, const void* wd, int wid) {
  Rl t; t.on = task < 164;
  const bool att = task >= 144;
  const int ta = task - 144, sa = ta / 5, a = ta - 5 * sa;
  t.rs = att ? mk_rsrc(wo, 576 * 1152) : mk_rsrc(wd, 4608 * 1152);
  const int r0 = !att ? 32 * task + 4 * wid : (a < 4 ? 18 * (8 * sa + 2 * a) + 4 * wid : 18 * (8 * sa + wid) + 16);
  t.r0 = (unsigned)r0 * 1152u;
  t.rs4 = (!att ? 16u : (a < 4 ? 18u : 72u)) * 1152u;
  const int sidx = !att ? task / 36 : sa, ksi = !att ? task - 36 * sidx : 36 + a;
  t.dst = (unsigned)(((wid * 4 + sidx) * 44 + ksi) * 9216);
  return t;
}
DI void relayout(int task, const void* wo, const void* wd, char* ws, LAS char* lds, int lane, int wid) {
  const Rl t = rl_task(task, wo, wd, wid);
  if (!t.on) return;
  const unsigned ldsb = (unsigned)(size_t)lds + (unsigned)wid * 9216u;
  asm volatile("s_waitcnt lgkmcnt(0)" ::: "memory");
  bar();
#pragma unroll
  for (int i = 0; i < 9; ++i) {
    const int n = 64 * i + lane, jx = n / 72, c16 = n - 72 * jx;
    dma16(ldsb + (unsigned)i * 1024u, (unsigned)((jx & 3) * 1152 + c16 * 16) + (unsigned)(jx >> 2) * t.rs4, t.rs, t.r0);
  }
  wait_vm<0>();
  bar();
  rsrc_t rw = mk_rsrc(ws + WS_WP, (unsigned)WP_BYTES);
  const int g = lane >> 4, q = (lane >> 2) & 3, p = lane & 3;
  LAS char* base = lds + (8 * g + q) * 1152 + wid * 288;
#pragma unroll
  for (int u = 0; u < 9; ++u) {
    const int nb = u < 8 ? 64 * (u >> 1) + 16 * p + 8 * (u & 1) : 256 + 8 * p;
    bf16x8 f = cat(trd(base + nb), trd(base + nb + 4 * 1152));
    __builtin_amdgcn_raw_buffer_store_b128(__builtin_bit_cast(u32x4, f), rw, (unsigned)lane * 16u, t.dst + (unsigned)(u * 1024), 0);
  }
}
template <int P, int E> DI void ffn_egroup(Eng<8>& c, rsrc_t rh, unsigned hvoff, unsigned hsoff) {
  constexpr int tt = E >> 1, gi = E & 1;
  if constexpr (E == 3 && P < 2) eng_prime<8, 9 * (P + 1)>(c);
  u32x4 o = gelu_mul8(c.acc[tt][2 * gi], c.acc[tt][2 * gi + 1], c.acc[tt][4 + 2 * gi], c.acc[tt][4 + 2 * gi + 1]);
  __builtin_amdgcn_raw_buffer_store_b128(o, rh, hvoff, hsoff + (unsigned)(P * 2048 + tt * 144 * 1024 + gi * 1024), H_AUX);
  SB();
}
template <int P> DI void ffn_pass(Eng<8>& c, rsrc_t rh, unsigned hvoff, unsigned hsoff, char* ws, int lane, int wid) {
  eng_pass<8, P>(c);
  ffn_egroup<P, 0>(c, rh, hvoff, hsoff); ffn_egroup<P, 1>(c, rh, hvoff, hsoff);
  ffn_egroup<P, 2>(c, rh, hvoff, hsoff); ffn_egroup<P, 3>(c, rh, hvoff, hsoff);
  STAMP(4 + P * 4);
}
DI void k1_ffn(const void* x, const void* wg, const void* wu, const void* wo, const void* wd, char* ws, LAS char* lds, int xcd, int idx) {
  const int lane = threadIdx.x & 63, wid = rfl(threadIdx.x >> 6);
  const int s = xcd >> 1, hm = xcd & 1, jj = idx % 6, ml = idx / 6, mt = hm * 8 + ml, j = 6 * s + jj;
  Eng<8> c;
  c.r0 = mk_rsrc(wg, 576 * 9216); c.r1 = mk_rsrc(wu, 576 * 9216);
  rsrc_t rh = mk_rsrc(ws + WS_H, (unsigned)H_BYTES);
  const unsigned hvoff = (unsigned)lane * 16u, hsoff = (unsigned)(((mt * 8 + wid * 2) * 144 + j * 6) * 1024);
  c.colb = (unsigned)j * 384u;
  {
    int g = lane >> 4, q = (lane >> 2) & 3, p = lane & 3, swz = (q >> 1) | ((g & 1) << 1);
#pragma unroll
    for (int i = 0; i < 4; ++i) { c.rb[i] = lds + 1024 * g + 128 * q + 8 * p + ((i ^ swz) * 32); asm volatile("" : "+v"(c.rb[i])); }
  }
  c.stv = 0; c.dummy = 0;
  STAMP(0); RSTAMP(13);
  c.rot = ROT_ON ? ((0x7420 >> (4 * (ml & 3)) & 15) + (0x865320 >> (4 * jj) & 15)) % 9 : 0;
  if (ml >= 4) { for (int i = 0; i < S2_N; ++i) __builtin_amdgcn_s_sleep(S2_SLEEP); }
  eng_start<8>(c, x, lds, mt, lane, wid);
  ffn_pass<0>(c, rh, hvoff, hsoff, ws, lane, wid); ffn_pass<1>(c, rh, hvoff, hsoff, ws, lane, wid); ffn_pass<2>(c, rh, hvoff, hsoff, ws, lane, wid);
  if (ml < 4) {
    __builtin_amdgcn_s_setprio(0);
    relayout(idx * 8 + xcd, wo, wd, ws, lds, lane, wid);
    const int un = (xcd * 24 + idx) * 2; fill_empty(ws, un, lane, wid); fill_empty(ws, un + 1, lane, wid);
  }
}

constexpr int KVP = 144;
template <int P> DI void aux_epi(Eng<5>& c, rsrc_t rpq, const unsigned (&pqo)[8], LAS char* img, int quad) {
  mfma_fence();
  if constexpr (P < 2) eng_prime<5, 9 * (P + 1)>(c);
#pragma unroll
  for (int tt = 0; tt < 2; ++tt) {
#pragma unroll
    for (int t = 0; t < 5; ++t) {
      f32x4 v = c.acc[tt][t];
      u32x2 o;
      if constexpr (P < 2) { o[0] = pk2(phi(v[0]), phi(v[1])); o[1] = pk2(phi(v[2]), phi(v[3])); }
      else { o[0] = pk2(v[0], v[1]); o[1] = pk2(v[2], v[3]); }
      if constexpr (P == 0) {
        __builtin_amdgcn_raw_buffer_store_b64(o, rpq, pqo[t], (unsigned)(tt * 32 * 768), 0);
        if (t == 1) __builtin_amdgcn_raw_buffer_store_b64(o, rpq, pqo[5], (unsigned)(tt * 32 * 768), 0);
        if (t == 3) __builtin_amdgcn_raw_buffer_store_b64(o, rpq, pqo[6], (unsigned)(tt * 32 * 768), 0);
      } else if (t < 4 || quad < 2) *(LAS u32x2*)(img + tt * 16 * KVP + t * 32) = o;
    }
    if constexpr (P == 0) { const u32x2 z = {0u, 0u}; __builtin_amdgcn_raw_buffer_store_b64(z, rpq, pqo[7], (unsigned)(tt * 32 * 768), 0); }
  }
  mfma_fence();
}
DI void k1_aux(const void* x, const void* wqkv, const void* wo, const void* wd, const void* gw, char* ws, LAS char* lds, int xcd, int a) {
  const int lane = threadIdx.x & 63, wid = rfl(threadIdx.x >> 6);
  const int s = xcd >> 1, hm = xcd & 1, mt = hm * 8 + (a & 7), hg = 2 * s + (a >> 3), auxid = xcd * 16 + a;
  const int g = lane >> 4, q = (lane >> 2) & 3, p = lane & 3, t16 = lane & 15;
  Eng<5> c;
  c.r0 = mk_rsrc(wqkv, 576 * 3456); c.r1 = c.r0;
  c.rot = ROT_ON ? (a & 7) + (a >> 3) : 0;
  c.colb = (unsigned)hg * 128u;
  {
    int swz = (q >> 1) | ((g & 1) << 1);
#pragma unroll
    for (int t = 0; t < 5; ++t) {
      int b = 16 * hg + 32 * t + 8 * p; b = b > 248 ? 248 : b;
      c.rb[t] = lds + (b >> 7) * 4096 + 1024 * g + 128 * q + ((((b >> 5) & 3) ^ swz) * 32) + (b & 31);
      asm volatile("" : "+v"(c.rb[t]));
    }
  }
  rsrc_t rpq = mk_rsrc(ws + WS_PQ, (unsigned)PQ_BYTES);
  unsigned pqo[8];
  {
    const unsigned base = (unsigned)(((mt * 8 + wid * 2) * 32 + 4 * hg) * 768 + t16 * 16);
#pragma unroll
    for (int t = 0; t < 5; ++t) {
      const int ci = 4 * t + g, hh = ci < 5 ? 0 : ci < 9 ? 1 : ci < 14 ? 2 : 3, sc = ci - (hh == 0 ? 0 : hh == 1 ? 4 : hh == 2 ? 9 : 13);
      pqo[t] = ci < 18 ? base + (unsigned)(hh * 768 + (sc >> 1) * 256 + (sc & 1) * 8) : OOB;
    }
    pqo[5] = g == 0 ? base + 768u : OOB;
    pqo[6] = g == 1 ? base + 3u * 768u : OOB;
    pqo[7] = base + (unsigned)(g * 768 + 512 + 8);
  }
  LAS char* imgK = lds + XOFF;
  LAS char* imgV = lds;
  const int imgo = (wid * 32 + t16) * KVP + g * 8;
  __builtin_amdgcn_s_setprio(AUX_PRIO);
  c.stv = 0; c.dummy = 0;
  STAMP(0); RSTAMP(13);
  {
    rsrc_t rx = mk_rsrc(x, 2048 * 1152);
    const unsigned v = lane < 36 ? (unsigned)(((hm * 8 + 4) * 128 + a * 32 + wid * 8) * 1152 + lane * 128) : OOB;
    touch(c.dummy, v, rx, 0u); touch(c.dummy, v, rx, 4608u);
    wait_vm<0>(); pin(c.dummy);
  }
  eng_start<5>(c, x, lds, mt, lane, wid);
  eng_pass<5, 0>(c); aux_epi<0>(c, rpq, pqo, nullptr, g); STAMP(4);
  eng_pass<5, 1>(c);
  aux_epi<1>(c, rpq, pqo, imgK + imgo, g); STAMP(8);
  eng_pass<5, 2>(c);
  asm volatile("s_waitcnt lgkmcnt(0)" ::: "memory");
  bar();
  aux_epi<2>(c, rpq, pqo, imgV + imgo, g);
  asm volatile("s_waitcnt vmcnt(0) lgkmcnt(0)" ::: "memory");
  bar();
  if (hg == 0) {
    f32x4 l0 = {0.f, 0.f, 0.f, 0.f}, l1 = {0.f, 0.f, 0.f, 0.f};
    bf16x8 gv[18];
#pragma unroll
    for (int i = 0; i < 18; ++i) gv[i] = *(const bf16x8*)((const char*)gw + kc_of(c.rot, i >> 1) * 128 + g * 32 + (i & 1) * 16);
    SB();
#pragma unroll
    for (int cc = 0; cc < 9; ++cc)
#pragma unroll
      for (int ks = 0; ks < 2; ++ks) {
        const bf16x8 w = gv[2 * cc + ks];
        l0 = mfma(w, __builtin_bit_cast(bf16x8, c.x[cc][ks]), l0);
        l1 = mfma(w, __builtin_bit_cast(bf16x8, c.x[cc][2 + ks]), l1);
      }
    const float l = (lane & 16) ? l1[0] : l0[0];
    if (lane < 32) ((float*)(ws + WS_G))[mt * 128 + wid * 32 + lane] = 1.0f / (1.0f + __expf(-l));
  }
  {
    const int h = 4 * hg + wid, c0 = (18 * wid) & ~3, off = (18 * wid) & 3;
    f32x4 kv[2][2] = {}, z[2] = {};
    const u32x4 one4 = {0x3f803f80u, 0x3f803f80u, 0x3f803f80u, 0x3f803f80u};
    const bf16x8 ones = __builtin_bit_cast(bf16x8, one4);
    const int ro = (8 * g + q) * KVP + c0 * 2 + p * 8;
#pragma unroll
    for (int ks = 0; ks < 4; ++ks) {
      bf16x8 a[2], b[2];
#pragma unroll
      for (int i = 0; i < 2; ++i) {
        a[i] = cat(trd(imgK + ro + ks * 32 * KVP + i * 32), trd(imgK + ro + ks * 32 * KVP + i * 32 + 4 * KVP));
        b[i] = cat(trd(imgV + ro + ks * 32 * KVP + i * 32), trd(imgV + ro + ks * 32 * KVP + i * 32 + 4 * KVP));
      }
#pragma unroll
      for (int i = 0; i < 2; ++i) {
        kv[i][0] = mfma(a[i], b[0], kv[i][0]); kv[i][1] = mfma(a[i], b[1], kv[i][1]); z[i] = mfma(a[i], ones, z[i]);
      }
    }
    rsrc_t rkv = mk_rsrc(ws + WS_KV, (unsigned)KV_BYTES);
    const unsigned so = (unsigned)((mt * 32 + h) * (int)KV_HEAD);
#pragma unroll
    for (int i = 0; i < 2; ++i) {
      const bool dok = i == 0 || g == 0;
#pragma unroll
      for (int jb = 0; jb < 2; ++jb) {
        const int e = 16 * jb + t16 - off;
        const bool ok = dok && e >= 0 && e < 18;
        __builtin_amdgcn_raw_buffer_store_b128(__builtin_bit_cast(u32x4, kv[i][jb]), rkv, ok ? (unsigned)((e * 24 + 16 * i + 4 * g) * 4) : OOB, so, 0);
      }
      __builtin_amdgcn_raw_buffer_store_b128(__builtin_bit_cast(u32x4, z[i]), rkv, (dok && t16 == 0) ? (unsigned)((18 * 24 + 16 * i + 4 * g) * 4) : OOB, so, 0);
    }
  }
  STAMP(12);
  fill_empty(ws, 384 + auxid, lane, wid);
  if (auxid < 16) { const u32x4 e = {EMPTY, EMPTY, EMPTY, EMPTY}; *(u32x4*)(ws + WS_KVF + (size_t)auxid * 4096 + threadIdx.x * 16) = e; }
}

constexpr int BLK = 36864, PF2 = 4, KAOFF = 4 * BLK, NU = 36;
constexpr int MID8 = 14;
constexpr Ev ev2_at(int S, int I) {
  if (S < 0) return Ev{2, I >> 3, I & 7};
  if ((I & 3) == 1 && I < 32 && S + 2 <= 8) return Ev{2, S + 2, I >> 2};
  if (S == 7 && (I == 3 || I == 7)) return Ev{7, 0, I >> 2};
  return Ev{0, 0, 0};
}
constexpr int npos2(int S) { return S < 0 ? 16 : (S == 10 ? 9 : NU); }
constexpr int younger2(int kind, int id, int piece, int S, int I) {
  int cnt = 0; bool seen = false;
  for (int n = -1; n <= S; ++n)
    for (int i = 0; i < (n == S ? I : npos2(n)); ++i) {
      Ev e = ev2_at(n, i);
      if (e.kind == kind && e.id == id && e.piece == piece) { cnt = 0; seen = true; } else if (e.kind != 0) ++cnt;
    }
  return seen ? cnt : -1;
}
struct G2 {
  int stv;
  rsrc_t rh, rg;
  unsigned l16, hsoff, goff;
  LAS char* rb[3];
  LAS char* obr;
  u32x4 h[3][8];
  unsigned gt[2];
  f32x4 acc[2][9];
  bf16x8 wf[PF2 + 1];
};
DI bool has_empty(const u32x4& v) { return v[0] == EMPTY || v[1] == EMPTY || v[2] == EMPTY || v[3] == EMPTY; }
template <int S, int I> DI void g2_event(G2& c) {
  constexpr Ev e = ev2_at(S, I);
  if constexpr (e.kind == 2) ld16(c.h[e.id % 3][e.piece], c.l16, c.rh, c.hsoff + (unsigned)((e.id * 4 + (e.piece >> 1)) * 1024 + (e.piece & 1) * 144 * 1024));
  else if constexpr (e.kind == 7) ld4(c.gt[e.piece], c.goff, c.rg, (unsigned)(e.piece * 64));
}
template <int S, int I> DI bf16x8 g2_rd(G2& c) {
  constexpr int o = (S % 4) * BLK + I * 1024;
  return *(LAS bf16x8*)(c.rb[o / 55296] + o % 55296);
}
template <int S, int I> DI void g2_pref(G2& c) {
  constexpr int B = (S * NU + I + PF2) % (PF2 + 1);
  if constexpr (I + PF2 < npos2(S)) c.wf[B] = g2_rd<S, I + PF2>(c);
  else if constexpr (S < 10) c.wf[B] = g2_rd<S + 1, I + PF2 - NU>(c);
}
template <int S, int I> DI void g2_unit(G2& c) {
  constexpr int ks = I / 9, u = I % 9, B = (S * NU + I) % (PF2 + 1);
  if constexpr (u == 0 && S <= 8) {
    wait_vm<younger2(2, S, 2 * ks + 1, S, I)>();
    pin(c.h[S % 3][2 * ks]); pin(c.h[S % 3][2 * ks + 1]);
  }
  if constexpr (S == 8 && I == NU - PF2 + 1) {
#pragma unroll
    for (int i = 0; i < 8; ++i) c.h[0][i] = *(LAS u32x4*)(c.obr + i * 1024);
    c.h[1][0] = *(LAS u32x4*)(c.obr + 8192); c.h[1][1] = *(LAS u32x4*)(c.obr + 9216);
    SB();
  }
  if constexpr (S == 9 && I == 0) {
    wait_vm<0>(); pin(c.gt[0]); pin(c.gt[1]);
    mfma_fence();
#pragma unroll
    for (int tt = 0; tt < 2; ++tt) {
      const float og = 1.0f - __builtin_bit_cast(float, c.gt[tt]);
#pragma unroll
      for (int k = 0; k < 9; ++k) c.acc[tt][k] *= og;
    }
    mfma_fence();
  }
  if constexpr (S == 8 && I == MID8) bar();
  if constexpr (I == NU - PF2 && S < 10) bar();
  bf16x8 w = c.wf[B];
  const u32x4 b0 = c.h[S % 3][2 * ks], b1 = c.h[S % 3][2 * ks + 1];
  if constexpr (S == 0 && ks == 0) mfma_zero(c.acc[0][u], w, b0); else mfma_acc(c.acc[0][u], w, b0);
  SB(); g2_pref<S, I>(c); SB();
  if constexpr (S == 0 && ks == 0) mfma_zero(c.acc[1][u], w, b1); else mfma_acc(c.acc[1][u], w, b1);
  SB(); g2_event<S, I>(c); SB();
}
template <int S, int... Is> DI void g2_units(G2& c, std::integer_sequence<int, Is...>) { (g2_unit<S, Is>(c), ...); }
template <int S> DI void g2_step(G2& c) { g2_units<S>(c, std::make_integer_sequence<int, npos2(S)>{}); }
template <int... Is> DI void g2_prologue(G2& c, std::integer_sequence<int, Is...>) { (g2_event<-1, Is>(c), ...); }
DI f32x4 up_lo(unsigned a, unsigned b) {
  return f32x4{__builtin_bit_cast(float, a << 16), __builtin_bit_cast(float, a & 0xffff0000u), __builtin_bit_cast(float, b << 16), __builtin_bit_cast(float, b & 0xffff0000u)};
}
DI int ob_off(int tq) { return tq < 3 ? 3 * BLK + tq * 10240 : 2 * BLK + 9216; }

DI void k2_helper(char* ws, LAS char* lds, int hw, int lane, int s, int m, int ng) {
  rsrc_t rw = mk_rsrc(ws + WS_WP, (unsigned)WP_BYTES), rk = mk_rsrc(ws + WS_KVF, (unsigned)KVF_BYTES);
  const unsigned l16 = (unsigned)lane * 16u, m0w = (unsigned)(size_t)lds + (unsigned)hw * 9216u;
  const unsigned wsoff = (unsigned)((ng * 4 + s) * 44 * 9216 + hw * 9216);
  const int t16 = lane & 15, quad = lane >> 4;
  auto blk = [&](int b) {
    const unsigned so = (b < 10 || hw == 0) ? wsoff + (unsigned)b * BLK : OOB;
    const unsigned m0v = m0w + (unsigned)(b & 3) * BLK;
    if (b < 10 || hw == 0) {
#pragma unroll
      for (int i = 0; i < 9; ++i) dma16(m0v + (unsigned)i * 1024u, l16, rw, so + (unsigned)i * 1024u);
    }
  };
  __builtin_amdgcn_s_setprio(HELP_PRIO);
  blk(0); blk(1);
  wait_vm<9>(); bar();
  blk(2);
  if (blockIdx.x < 128 && hw == 3) {
    const int rh_ = blockIdx.x >> 2, rph = (blockIdx.x >> 1) & 1, rhalf = blockIdx.x & 1;
    int e = t16;
    if (rph) e = (t16 & 3) == 2 ? 18 : (((t16 >> 2) == (rh_ & 3) && (t16 & 3) < 2) ? 16 + (t16 & 3) : -1);
    const bool ok = e >= 0 && 2 * quad + rhalf < 5;
    rsrc_t rkv = mk_rsrc(ws + WS_KV, (unsigned)KV_BYTES);
    const unsigned vo = ok ? (unsigned)(rh_ * (int)KV_HEAD + (e * 24 + 8 * quad + 4 * rhalf) * 4) : OOB;
    u32x4 kp[16];
#pragma unroll
    for (int i = 0; i < 16; ++i) ld16(kp[i], vo, rkv, (unsigned)(i * 32 * (int)KV_HEAD));
    wait_vm<25>(); bar();
    blk(3);
    wait_vm<9>();
    f32x4 a = {0.f, 0.f, 0.f, 0.f};
#pragma unroll
    for (int i = 0; i < 16; ++i) { pin(kp[i]); a += __builtin_bit_cast(f32x4, kp[i]); }
    const int d0 = 8 * quad + 4 * rhalf - ((18 * rh_) & 3);
#pragma unroll
    for (int j = 0; j < 4; ++j) a[j] = (d0 + j >= 0 && d0 + j < 18) ? a[j] : 0.f;
    const u32x2 o = {pk2(a[0], a[1]), pk2(a[2], a[3])};
    __builtin_amdgcn_raw_buffer_store_b64(o, rk, l16 + (unsigned)rhalf * 8u, (unsigned)(blockIdx.x >> 1) * 1024u, 16);
    bar();
  } else {
    wait_vm<9>(); bar();
    blk(3);
    wait_vm<9>(); bar();
  }
  blk(4); wait_vm<9>(); bar();
  blk(5); wait_vm<9>(); bar();
  blk(6);
  u32x4 ka4[4];
  const unsigned koff = (unsigned)((8 * s + 2 * hw) * 2048);
#pragma unroll
  for (int i = 0; i < 4; ++i) ld16_sc1(ka4[i], l16, rk, koff + (unsigned)i * 1024u);
  wait_vm<13>(); bar();
  blk(7);
  wait_vm<9>();
#pragma unroll
  for (int i = 0; i < 4; ++i) pin(ka4[i]);
  {
    unsigned spins = 0;
    while (__builtin_amdgcn_ballot_w64(has_empty(ka4[0]) || has_empty(ka4[1]) || has_empty(ka4[2]) || has_empty(ka4[3])) != 0ull && ++spins < (1u << 18)) {
      __builtin_amdgcn_s_sleep(2);
      asm volatile("buffer_load_dwordx4 %0, %4, %5, %6 offen sc1\n\tbuffer_load_dwordx4 %1, %4, %5, %6 offen offset:1024 sc1\n\t"
                   "buffer_load_dwordx4 %2, %4, %5, %6 offen offset:2048 sc1\n\tbuffer_load_dwordx4 %3, %4, %5, %6 offen offset:3072 sc1\n\ts_waitcnt vmcnt(0)"
                   : "=&v"(ka4[0]), "=&v"(ka4[1]), "=&v"(ka4[2]), "=&v"(ka4[3]) : "v"(l16), "s"(rk), "s"(koff) : "memory");
    }
  }
#pragma unroll
  for (int i = 0; i < 4; ++i) *(LAS u32x4*)(lds + KAOFF + (hw * 4 + i) * 1024 + lane * 16) = ka4[i];
  rsrc_t rpq = mk_rsrc(ws + WS_PQ, (unsigned)PQ_BYTES), rg = mk_rsrc(ws + WS_G, (unsigned)G_BYTES);
  const unsigned pqv = quad < 3 ? l16 : OOB, pqs = (unsigned)(((m * 8 + hw * 2) * 32 + 8 * s) * 768);
  u32x4 pq[16]; unsigned gt[2];
#pragma unroll
  for (int i = 0; i < 16; ++i) ld16(pq[i], pqv, rpq, pqs + (unsigned)((i >> 1) * 768 + (i & 1) * 32 * 768));
  ld4(gt[0], (unsigned)((m * 128 + hw * 32 + t16) * 4), rg, 0u); ld4(gt[1], (unsigned)((m * 128 + hw * 32 + t16) * 4), rg, 64u);
  asm volatile("s_waitcnt lgkmcnt(0)" ::: "memory");
  bar();
  blk(8);
  wait_vm<9>();
#pragma unroll
  for (int i = 0; i < 16; ++i) pin(pq[i]);
  pin(gt[0]); pin(gt[1]);
  bar();
  blk(9);
  u32x4 ob[5][2];
#pragma unroll
  for (int tt = 0; tt < 2; ++tt) {
    const float g = __builtin_bit_cast(float, gt[tt]);
    float l0[2] = {0.f, 0.f}, l1[2] = {0.f, 0.f};
#pragma unroll
    for (int hh = 0; hh < 8; ++hh) {
      const f32x4 z = {0.f, 0.f, 0.f, 0.f};
      const bf16x8 b = __builtin_bit_cast(bf16x8, pq[2 * hh + tt]);
      const bf16x8 a0 = *(LAS bf16x8*)(lds + KAOFF + (2 * hh) * 1024 + lane * 16), a1 = *(LAS bf16x8*)(lds + KAOFF + (2 * hh + 1) * 1024 + lane * 16);
      f32x4 n1 = mfma(a0, b, z), n2 = mfma(a1, b, z);
      const float sc = g * __builtin_amdgcn_rcpf(n2[2] + 1e-6f);
      ob[hh >> 1][tt][2 * (hh & 1)] = pk2(n1[0] * sc, n1[1] * sc);
      ob[hh >> 1][tt][2 * (hh & 1) + 1] = pk2(n1[2] * sc, n1[3] * sc);
      l0[hh >> 2] += n2[0] * sc; l1[hh >> 2] += n2[1] * sc;
    }
    ob[4][tt] = u32x4{pk2(l0[0], l1[0]), 0u, pk2(l0[1], l1[1]), 0u};
    if (tt == 0) { SB(); wait_vm<9>(); bar(); blk(10); SB(); }
  }
  wait_vm<0>();
  asm volatile("s_waitcnt lgkmcnt(0)" ::: "memory");
  bar();
#pragma unroll
  for (int k = 0; k < 5; ++k)
#pragma unroll
    for (int tt = 0; tt < 2; ++tt) *(LAS u32x4*)(lds + ob_off(hw) + (2 * k + tt) * 1024 + lane * 16) = ob[k][tt];
  asm volatile("s_waitcnt lgkmcnt(0)" ::: "memory");
  bar();
  bar();
}

DI void k2_body(char* ws, void* out, LAS char* lds) {
  const int lane = threadIdx.x & 63, wid = rfl(threadIdx.x >> 6);
  const int xcd = blockIdx.x & 7, idx = blockIdx.x >> 3, s = xcd >> 1, hm = xcd & 1, m = hm * 8 + (idx >> 2), ng = idx & 3;
  if (wid >= 4) { k2_helper(ws, lds, wid - 4, lane, s, m, ng); return; }
  const int tq = wid, t16 = lane & 15, quad = lane >> 4, tile = m * 4 + ng;
  G2 c;
  c.stv = 0;
  c.rh = mk_rsrc(ws + WS_H, (unsigned)H_BYTES); c.rg = mk_rsrc(ws + WS_G, (unsigned)G_BYTES);
  c.l16 = (unsigned)lane * 16u;
  c.hsoff = (unsigned)(((m * 8 + tq * 2) * 144 + 36 * s) * 1024);
  c.goff = (unsigned)((m * 128 + tq * 32 + t16) * 4);
  c.obr = lds + ob_off(tq) + lane * 16;
#pragma unroll
  for (int i = 0; i < 3; ++i) { c.rb[i] = lds + lane * 16 + i * 55296; asm volatile("" : "+v"(c.rb[i])); }
  STAMP(0); RSTAMP(13);
  g2_prologue(c, std::make_integer_sequence<int, 16>{});
  bar();
  c.wf[0] = g2_rd<0, 0>(c); c.wf[1] = g2_rd<0, 1>(c); c.wf[2] = g2_rd<0, 2>(c); c.wf[3] = g2_rd<0, 3>(c);
  SB();
  STAMP(1);
  g2_step<0>(c); g2_step<1>(c);
  STAMP(2);
  g2_step<2>(c); g2_step<3>(c); STAMP(9); g2_step<4>(c); g2_step<5>(c); STAMP(10); g2_step<6>(c); STAMP(11); g2_step<7>(c); STAMP(12); g2_step<8>(c);
  STAMP(3);
  g2_step<9>(c); g2_step<10>(c);
  mfma_fence();
  STAMP(4); STAMP(5);
  rsrc_t rxc = mk_rsrc(ws + WS_X, (unsigned)X_BYTES);
  const unsigned xo = (unsigned)(tile * 8 + tq * 2) * XUNIT;
  const bool own8 = tq == s;
  f32x4 r0[2], r1[2], r8[2];
#pragma unroll
  for (int tt = 0; tt < 2; ++tt) {
#pragma unroll
    for (int P = 0; P < 4; ++P) {
      const f32x4 p0 = c.acc[tt][2 * P], p1 = c.acc[tt][2 * P + 1];
      const u32x4 o = {pk2(p0[0], p0[1]), pk2(p0[2], p0[3]), pk2(p1[0], p1[1]), pk2(p1[2], p1[3])};
      __builtin_amdgcn_raw_buffer_store_b128(o, rxc, c.l16, P == s ? OOB : xo + (unsigned)(tt * XUNIT + (P * 3 + s - (s > P ? 1 : 0)) * 1024), 16);
    }
    const u32x2 o = {pk2(c.acc[tt][8][0], c.acc[tt][8][1]), pk2(c.acc[tt][8][2], c.acc[tt][8][3])};
    __builtin_amdgcn_raw_buffer_store_b64(o, rxc, (unsigned)lane * 8u, own8 ? OOB : xo + (unsigned)(tt * XUNIT + 12288 + (s - (s > tq ? 1 : 0)) * 512), 16);
    r0[tt] = c.acc[tt][0]; r1[tt] = c.acc[tt][1]; r8[tt] = c.acc[tt][8];
#pragma unroll
    for (int P = 1; P < 4; ++P) if (s == P) { r0[tt] = c.acc[tt][2 * P]; r1[tt] = c.acc[tt][2 * P + 1]; }
  }
  STAMP(6);
  {
    u32x4 pv[2][3]; u32x2 p8[2][3];
    const unsigned l8 = (unsigned)lane * 8u;
    unsigned spins = 0;
    bool bad;
    do {
      bad = false;
#pragma unroll
      for (int tt = 0; tt < 2; ++tt) {
        const unsigned so = xo + (unsigned)(tt * XUNIT + s * 3072), s8 = own8 ? xo + (unsigned)(tt * XUNIT + 12288) : OOB;
        asm volatile("buffer_load_dwordx4 %0, %6, %8, %9 offen sc1\n\tbuffer_load_dwordx4 %1, %6, %8, %9 offen offset:1024 sc1\n\t"
                     "buffer_load_dwordx4 %2, %6, %8, %9 offen offset:2048 sc1\n\tbuffer_load_dwordx2 %3, %7, %8, %10 offen sc1\n\t"
                     "buffer_load_dwordx2 %4, %7, %8, %10 offen offset:512 sc1\n\tbuffer_load_dwordx2 %5, %7, %8, %10 offen offset:1024 sc1"
                     : "=&v"(pv[tt][0]), "=&v"(pv[tt][1]), "=&v"(pv[tt][2]), "=&v"(p8[tt][0]), "=&v"(p8[tt][1]), "=&v"(p8[tt][2])
                     : "v"(c.l16), "v"(l8), "s"(rxc), "s"(so), "s"(s8) : "memory");
      }
      asm volatile("s_waitcnt vmcnt(0)"
                   : "+v"(pv[0][0]), "+v"(pv[0][1]), "+v"(pv[0][2]), "+v"(p8[0][0]), "+v"(p8[0][1]), "+v"(p8[0][2]),
                     "+v"(pv[1][0]), "+v"(pv[1][1]), "+v"(pv[1][2]), "+v"(p8[1][0]), "+v"(p8[1][1]), "+v"(p8[1][2]) :: "memory");
#pragma unroll
      for (int tt = 0; tt < 2; ++tt)
#pragma unroll
        for (int k = 0; k < 3; ++k) bad = bad || has_empty(pv[tt][k]) || p8[tt][k][0] == EMPTY || p8[tt][k][1] == EMPTY;
    } while (__builtin_amdgcn_ballot_w64(bad) != 0ull && ++spins < (1u << 18));
    STAMP(7);
#pragma unroll
    for (int tt = 0; tt < 2; ++tt) {
#pragma unroll
      for (int k = 0; k < 3; ++k) {
        r0[tt] += up_lo(pv[tt][k][0], pv[tt][k][1]); r1[tt] += up_lo(pv[tt][k][2], pv[tt][k][3]); r8[tt] += up_lo(p8[tt][k][0], p8[tt][k][1]);
      }
      char* orow = (char*)out + (size_t)(m * 128 + tq * 32 + tt * 16 + t16) * 1152 + ng * 288;
      u32x4 o = {pk2(r0[tt][0], r0[tt][1]), pk2(r0[tt][2], r0[tt][3]), pk2(r1[tt][0], r1[tt][1]), pk2(r1[tt][2], r1[tt][3])};
      *(u32x4*)(orow + s * 64 + quad * 16) = o;
      if (own8) { u32x2 o8 = {pk2(r8[tt][0], r8[tt][1]), pk2(r8[tt][2], r8[tt][3])}; *(u32x2*)(orow + 256 + quad * 8) = o8; }
    }
    STAMP(8);
  }
}
#endif

__global__ __launch_bounds__(256, 2) void k1(const void* x, const void* wqkv, const void* wo, const void* wg, const void* wu,
                                             const void* wd, const void* gw, char* ws) {
#if defined(__HIP_DEVICE_COMPILE__)
  extern __shared__ __attribute__((aligned(16))) char smem[];
  LAS char* lds = (LAS char*)smem;
  const int xcd = blockIdx.x & 7, idx = blockIdx.x >> 3;
  {
    unsigned long long pc; unsigned len;
    asm volatile("s_getpc_b64 %0\n.Lcode_beg_gm:\n\ts_mov_b32 %1, .Lcode_end_gm-.Lcode_beg_gm" : "=s"(pc), "=s"(len));
    if (idx < 32) {
      rsrc_t rc = mk_rsrc((const void*)pc, len & ~3u);
      unsigned d = 0;
      touch(d, (unsigned)((threadIdx.x >> 6) * 32 + idx) * 4096u + (threadIdx.x & 63) * 64u, rc, 0u);
      wait_vm<0>();
      pin(d);
    }
  }
  const int lo = idx & 31, hi = idx >> 5;
  if (lo < 24) k1_ffn(x, wg, wu, wo, wd, ws, lds, xcd, hi * 24 + lo);
  else k1_aux(x, wqkv, wo, wd, gw, ws, lds, xcd, hi * 8 + lo - 24);
#endif
}
__global__ __launch_bounds__(512, 2) void k2(char* ws, void* out) {
#if defined(__HIP_DEVICE_COMPILE__)
  extern __shared__ __attribute__((aligned(16))) char smem[];
  k2_body(ws, out, (LAS char*)smem);
  asm volatile(".Lcode_end_gm:");
#endif
}

extern "C" void kernel_launch(void* const* d_in, const int* in_sizes, int n_in, void* d_out, int out_size, void* d_ws, size_t ws_size,
                              hipStream_t stream) {
  (void)hipFuncSetAttribute((const void*)k1, hipFuncAttributeMaxDynamicSharedMemorySize, K1_LDS);
  (void)hipFuncSetAttribute((const void*)k2, hipFuncAttributeMaxDynamicSharedMemorySize, K2_LDS);
  k1<<<512, 256, K1_LDS, stream>>>(d_in[0], d_in[1], d_in[2], d_in[4], d_in[5], d_in[6], d_in[7], (char*)d_ws);
  k2<<<256, 512, K2_LDS, stream>>>((char*)d_ws, d_out);
}
```

```cpp
#include <hip/hip_runtime.h>
#include <hip/hip_bf16.h>
#include <utility>

typedef __bf16 bf16x8 __attribute__((ext_vector_type(8)));
typedef __bf16 bf16x4 __attribute__((ext_vector_type(4)));
typedef __bf16 bf16x2 __attribute__((ext_vector_type(2)));
typedef float f32x4 __attribute__((ext_vector_type(4)));
typedef float f32x2 __attribute__((ext_vector_type(2)));
typedef unsigned u32x4 __attribute__((ext_vector_type(4)));
typedef unsigned u32x2 __attribute__((ext_vector_type(2)));
#define LAS __attribute__((address_space(3)))
#define DI __device__ __forceinline__
#define RSRC3 0x00020000

constexpr size_t WS_H = 0;
constexpr size_t H_BYTES = (size_t)128 * 144 * 1024;
constexpr size_t WS_OB = WS_H + H_BYTES;
constexpr size_t OB_BYTES = (size_t)128 * 20 * 1024;
constexpr size_t WS_KV = WS_OB + OB_BYTES;
constexpr size_t KV_BYTES = (size_t)32 * 16 * 2048;
constexpr size_t WS_G = WS_KV + KV_BYTES;
constexpr size_t G_BYTES = 4 * 2048 * 4;
constexpr size_t WS_X = WS_G + G_BYTES;
constexpr unsigned XUNIT = 13824;
constexpr size_t X_BYTES = (size_t)512 * XUNIT;
constexpr size_t WS_F = WS_X + X_BYTES;
constexpr unsigned F_EP = 0;
constexpr unsigned F_ST = 4096;
constexpr unsigned F_H = F_ST + 512 * 8;
constexpr unsigned F_K = F_H + 16 * 4 * 4 * 256;
constexpr unsigned F_X = F_K + 32 * 128;
constexpr unsigned F_BYTES = F_X + 64 * 4 * 64;
constexpr int K1_LDS = 81920;

#define PRIO_MODE 1
#define MAPB 1
#define AUX_PRIO 0
#define S2_SLEEP 70
#define S2_N 1
#define ROT_ON 1
#define CODE_PF 1
#define NT_W 0
#define NT_X 0
#define HELP_PRIO 3
#define P2_PRIO 0
#define SPIN_MAX (1u << 13)
#define H_AUX 16

#if defined(__HIP_DEVICE_COMPILE__)
typedef __amdgpu_buffer_rsrc_t rsrc_t;
constexpr unsigned OOB = 0x7ff00000u;
DI rsrc_t mk_rsrc(const void* p, unsigned bytes) { return __builtin_amdgcn_make_buffer_rsrc((void*)p, (short)0, (int)bytes, RSRC3); }
DI unsigned rfl(unsigned x) { return __builtin_amdgcn_readfirstlane(x); }
template <int N> DI void wait_vm() { static_assert(N >= 0 && N < 64, "vmcnt"); asm volatile("s_waitcnt vmcnt(%0)" ::"i"(N) : "memory"); }
DI void bar() { __builtin_amdgcn_s_barrier(); }
#define SB() __builtin_amdgcn_sched_barrier(0)
DI void dma16(unsigned m0v, unsigned voff, rsrc_t rs, unsigned soff) {
  asm volatile("s_mov_b32 m0, %0\n\ts_nop 0\n\tbuffer_load_dwordx4 %1, %2, %3 offen lds" ::"s"(m0v), "v"(voff), "s"(rs), "s"(soff) : "memory");
}
DI void dma16_nt(unsigned m0v, unsigned voff, rsrc_t rs, unsigned soff) {
  asm volatile("s_mov_b32 m0, %0\n\ts_nop 0\n\tbuffer_load_dwordx4 %1, %2, %3 offen nt lds" ::"s"(m0v), "v"(voff), "s"(rs), "s"(soff) : "memory");
}
DI void ld16(u32x4& dst, unsigned voff, rsrc_t rs, unsigned soff) {
  asm volatile("buffer_load_dwordx4 %0, %1, %2, %3 offen" : "=v"(dst) : "v"(voff), "s"(rs), "s"(soff) : "memory");
}
DI void ld16_sc1(u32x4& dst, unsigned voff, rsrc_t rs, unsigned soff) {
  asm volatile("buffer_load_dwordx4 %0, %1, %2, %3 offen sc1" : "=v"(dst) : "v"(voff), "s"(rs), "s"(soff) : "memory");
}
DI void ld4(unsigned& dst, unsigned voff, rsrc_t rs, unsigned soff) {
  asm volatile("buffer_load_dword %0, %1, %2, %3 offen" : "=v"(dst) : "v"(voff), "s"(rs), "s"(soff) : "memory");
}
DI void ld4_sc1(unsigned& dst, unsigned voff, rsrc_t rs, unsigned soff) {
  asm volatile("buffer_load_dword %0, %1, %2, %3 offen sc1" : "=v"(dst) : "v"(voff), "s"(rs), "s"(soff) : "memory");
}
DI void touch(unsigned& dst, unsigned voff, rsrc_t rs, unsigned soff) {
  asm volatile("buffer_load_dword %0, %1, %2, %3 offen" : "+v"(dst) : "v"(voff), "s"(rs), "s"(soff) : "memory");
}
DI void dma16_half(unsigned m0v, unsigned voff, rsrc_t rs, unsigned soff) {
  asm volatile("s_mov_b32 m0, %0\n\ts_mov_b32 exec_hi, 0\n\tbuffer_load_dwordx4 %1, %2, %3 offen lds\n\ts_mov_b32 exec_hi, -1"
               ::"s"(m0v), "v"(voff), "s"(rs), "s"(soff) : "memory");
}
DI void st16(u32x4 v, rsrc_t rs, unsigned voff, unsigned soff, int) {
  __builtin_amdgcn_raw_buffer_store_b128(v, rs, voff, soff, 16);
  asm volatile("s_nop 1" ::"v"(v) : "memory");
}
DI void pin(u32x4& a) { asm volatile("" : "+v"(a)); }
DI void pin(unsigned& a) { asm volatile("" : "+v"(a)); }
DI bf16x4 trd(LAS char* p) { return __builtin_amdgcn_ds_read_tr16_b64_v4bf16((LAS bf16x4*)p); }
DI bf16x8 cat(bf16x4 lo, bf16x4 hi) { return __builtin_shufflevector(lo, hi, 0, 1, 2, 3, 4, 5, 6, 7); }
DI f32x4 mfma(bf16x8 a, bf16x8 b, f32x4 c) { return __builtin_amdgcn_mfma_f32_16x16x32_bf16(a, b, c, 0, 0, 0); }
DI void mfma_acc(f32x4& c, bf16x8 a, u32x4 b) { asm volatile("v_mfma_f32_16x16x32_bf16 %0, %1, %2, %0" : "+v"(c) : "v"(a), "v"(b)); }
DI void mfma_zero(f32x4& c, bf16x8 a, u32x4 b) { asm volatile("v_mfma_f32_16x16x32_bf16 %0, %1, %2, 0" : "=&v"(c) : "v"(a), "v"(b)); }
DI void mfma_fence() { SB(); asm volatile("s_nop 7\n\ts_nop 1" ::: "memory"); SB(); }
DI unsigned pk2(float lo, float hi) { f32x2 v = {lo, hi}; return __builtin_bit_cast(unsigned, __builtin_convertvector(v, bf16x2)); }
DI u32x4 gelu_mul8(f32x4 ga, f32x4 gb, f32x4 ua, f32x4 ub) {
  const float c1 = -2.0f * 1.4426950408889634f * 0.7978845608028654f, c2 = c1 * 0.044715f;
  float t0, t1, t2, t3, t4, t5, t6, t7;
#define R8(X) X(8, 0, 16) X(9, 1, 17) X(10, 2, 18) X(11, 3, 19) X(12, 4, 20) X(13, 5, 21) X(14, 6, 22) X(15, 7, 23)
#define S1(g, t, u) "v_mul_f32 %" #t ", %" #g ", %" #g "\n\t"
#define S2(g, t, u) "v_fma_f32 %" #t ", %" #t ", %25, %24\n\t"
#define S3(g, t, u) "v_mul_f32 %" #t ", %" #t ", %" #g "\n\t"
#define S4(g, t, u) "v_exp_f32 %" #t ", %" #t "\n\t"
#define S5(g, t, u) "v_mul_f32 %" #t ", %" #t ", %" #u "\n\t"
#define S6(g, t, u) "v_add_f32 %" #t ", 1.0, %" #t "\n\t"
#define S7(g, t, u) "v_rcp_f32 %" #t ", %" #t "\n\t"
  asm volatile("s_nop 7\n\t" R8(S1) R8(S2) R8(S3) R8(S4) R8(S6) R8(S7) R8(S3) R8(S5)
               "v_cvt_pk_bf16_f32 %0, %0, %1\n\tv_cvt_pk_bf16_f32 %1, %2, %3\n\tv_cvt_pk_bf16_f32 %2, %4, %5\n\tv_cvt_pk_bf16_f32 %3, %6, %7"
               : "=&v"(t0), "=&v"(t1), "=&v"(t2), "=&v"(t3), "=&v"(t4), "=&v"(t5), "=&v"(t6), "=&v"(t7)
               : "v"(ga[0]), "v"(ga[1]), "v"(ga[2]), "v"(ga[3]), "v"(gb[0]), "v"(gb[1]), "v"(gb[2]), "v"(gb[3]),
                 "v"(ua[0]), "v"(ua[1]), "v"(ua[2]), "v"(ua[3]), "v"(ub[0]), "v"(ub[1]), "v"(ub[2]), "v"(ub[3]), "v"(c1), "s"(c2));
  u32x4 o = {__builtin_bit_cast(unsigned, t0), __builtin_bit_cast(unsigned, t1), __builtin_bit_cast(unsigned, t2), __builtin_bit_cast(unsigned, t3)};
  return o;
}
DI float phi(float t) { return t > 0.f ? t + 1.000001f : __builtin_amdgcn_exp2f(t * 1.4426950408889634f) + 1e-6f; }
struct Sync { rsrc_t rf; unsigned t0, t1; };
DI void ld8_sc1(u32x2& dst, unsigned voff, rsrc_t rs, unsigned soff) {
  asm volatile("buffer_load_dwordx2 %0, %1, %2, %3 offen sc1" : "=v"(dst) : "v"(voff), "s"(rs), "s"(soff) : "memory");
}
DI void pin(u32x2& a) { asm volatile("" : "+v"(a)); }
DI void make_tag(Sync& y, u32x2 seed) {
  const unsigned long long e = ((unsigned long long)rfl(seed[1]) << 32) | rfl(seed[0]);
  const unsigned long long t = e * 6364136223846793005ull + 1442695040888963407ull;
  y.t0 = (unsigned)t; y.t1 = (unsigned)(t >> 32);
}
DI void publish(const Sync& y, unsigned off, int lane) {
  const u32x2 t = {y.t0, y.t1};
  __builtin_amdgcn_raw_buffer_store_b64(t, y.rf, lane == 0 ? 0u : OOB, off, 16);
}
DI bool flags_missing(const Sync& y, u32x2 f, bool want) { return __builtin_amdgcn_ballot_w64(want && (f[0] != y.t0 || f[1] != y.t1)) != 0ull; }
DI void poll(const Sync& y, u32x2 f, bool want, unsigned voff, unsigned soff) {
  unsigned spins = 0;
  while (flags_missing(y, f, want) && ++spins < (1u << 20)) {
    __builtin_amdgcn_s_sleep(4);
    asm volatile("buffer_load_dwordx2 %0, %1, %2, %3 offen sc1\n\ts_waitcnt vmcnt(0)" : "=&v"(f) : "v"(voff), "s"(y.rf), "s"(soff) : "memory");
  }
}

#define STAMP(i)
#define RSTAMP(i)

constexpr int HSLOT = 8192;
constexpr int NH = 6, PF = 4;
constexpr int XOFF = NH * HSLOT;

struct Ev { int kind, id, piece; };
constexpr int PRO = 14, NT0 = 0;
template <int U> constexpr Ev ev_at(int N, int I) {
  if (N < 0) {
    if (I >= PRO) return Ev{1, 3 + (I - PRO) / 2, (I - PRO) & 1};
    constexpr int k[PRO] = {1, 1, 2, 2, 2, 2, 1, 1, 1, 1, 2, 2, 2, 2}, id[PRO] = {0, 0, 0, 0, 0, 0, 1, 1, 2, 2, 1, 1, 1, 1},
                  pc[PRO] = {0, 1, 0, 1, 2, 3, 0, 1, 0, 1, 0, 1, 2, 3};
    return Ev{k[I], id[I], pc[I]};
  }
  if (N == 0) {
    if (U == 8) {
      if (I < 4) return Ev{1, 3 + I / 2, I & 1};
      if (I == 5 || I == 7 || I == 8 || I == 10) return Ev{2, 2, I == 5 ? 0 : I == 7 ? 1 : I == 8 ? 2 : 3};
    }
  }
  if (I >= 2 * U) return Ev{3, N / 9, 0};
  if (N >= 9 && N % 9 == 3 && I == (U == 8 ? 1 : 0) && (U == 8 || N == 21)) return Ev{9, N / 9 - 1, 0};
  const int ks = I / U, t = I % U;
  if ((t == U - PF || t == U - PF + 2) && 2 * N + ks + 5 <= 53) return Ev{1, 2 * N + ks + 5, (t - (U - PF)) / 2};
  if (N + 2 <= 8) {
    if (U == 8 && I < 4) return Ev{2, N + 2, I};
    if (U == 5 && (I == 0 || I == 2 || I == 4 || I == 5)) return Ev{2, N + 2, I == 5 ? 3 : I / 2};
  }
  return Ev{0, 0, 0};
}
constexpr int eops(int P) { return 4; }
template <int U> constexpr int npos(int N) { return N < 0 ? (U == 8 ? PRO : PRO + 4) : (N % 9 == 8 && U == 8 ? 2 * U + eops(N / 9) : 2 * U); }
template <int U> constexpr int younger(int kind, int id, int N, int I) {
  int cnt = 0; bool seen = false;
  for (int n = -1; n <= N; ++n)
    for (int i = 0; i < (n == N ? I : npos<U>(n)); ++i) {
      Ev e = ev_at<U>(n, i);
      if (e.kind == kind && e.id == id) { cnt = 0; seen = true; } else if (e.kind != 0) ++cnt;
    }
  return seen ? cnt : -1;
}
template <int U> constexpr int xrd_at(int I) {
  if (U == 8) return I == 9 ? 0 : I == 10 ? 1 : I == 11 ? 2 : I == 13 ? 3 : -1;
  return I >= 6 ? I - 6 : -1;
}

template <int U> struct Eng {
  int stv;
  rsrc_t r0, r1, rx;
  unsigned wvoff, xvoff[2];
  unsigned m0w, m0x, colb, dummy, foff;
  int lane;
  Sync y;
  int rot;
  LAS char* rb[U == 8 ? 4 : 5];
  LAS char* xr[2];
  u32x4 x[9][4];
  f32x4 acc[2][U];
  bf16x4 wf[PF + 1][2];
};
constexpr unsigned ROWB(int U) { return U == 8 ? 9216u : 3456u; }
constexpr unsigned PASSB(int U, int P) { return U == 8 ? 128u * P : 1152u * ((P + 1) % 3); }
DI unsigned kc_of(int rot, int C) { const int kc = C + rot; return (unsigned)(kc >= 9 ? kc - 9 : kc); }
template <int U, int N, int I> DI void eng_event(Eng<U>& c) {
  constexpr Ev e = ev_at<U>(N, I);
  if constexpr (e.kind == 1) {
    constexpr int n = e.id >> 1, ks = e.id & 1;
    constexpr unsigned so = (unsigned)(ks * 8) * ROWB(U) + PASSB(U, n / 9) + (U == 5 ? e.piece * 128u : 0u);
    dma16(c.m0w + (unsigned)((e.id % NH) * HSLOT + e.piece * 4096), c.wvoff, e.piece ? c.r1 : c.r0, kc_of(c.rot, n % 9) * (64u * ROWB(U)) + c.colb + so);
  } else if constexpr (e.kind == 2) {
    dma16(c.m0x + (unsigned)((e.id & 1) * 4096 + e.piece * 1024), c.xvoff[e.piece & 1], c.rx, kc_of(c.rot, e.id) * 128u + (unsigned)(e.piece * 8 * 1152));
  } else if constexpr (e.kind == 9) {
    if constexpr (U == 8) { wait_vm<younger<U>(3, e.id, N, I)>(); publish(c.y, c.foff + (unsigned)(e.id * 48), c.lane); }
    else { wait_vm<0>(); publish(c.y, c.foff, c.lane); }
  }
}
template <int U, int N, int I, int HALF> DI bf16x4 eng_rd(Eng<U>& c) {
  constexpr int ks = I / U, tile = I % U;
  if constexpr (U == 8) return trd(c.rb[tile & 3] + ((2 * N + ks) % NH) * HSLOT + (tile >> 2) * 4096 + HALF * 512);
  else return trd(c.rb[tile] + ((2 * N + ks) % NH) * HSLOT + HALF * 512);
}
template <int U, int N, int I, int HALF> DI void eng_pref(Eng<U>& c) {
  constexpr int B = ((N % 9) * 2 * U + I + PF) % (PF + 1);
  if constexpr (I + PF < 2 * U) c.wf[B][HALF] = eng_rd<U, N, I + PF, HALF>(c);
  else if constexpr (N % 9 < 8) c.wf[B][HALF] = eng_rd<U, N + 1, I + PF - 2 * U, HALF>(c);
}
template <int U, int N, int... Is> DI void eng_prime_(Eng<U>& c, std::integer_sequence<int, Is...>) {
  ((c.wf[(Is >> 1) % (PF + 1)][Is & 1] = eng_rd<U, N, (Is >> 1), (Is & 1)>(c)), ...);
}
template <int U, int N> DI void eng_prime(Eng<U>& c) { eng_prime_<U, N>(c, std::make_integer_sequence<int, 2 * PF>{}); SB(); }
template <int U, int M, int R> DI void eng_xrd(Eng<U>& c) {
  c.x[M][R] = *(LAS u32x4*)(c.xr[R & 1] + (M & 1) * 4096 + (R >> 1) * 2048);
}
template <int U, int N, int I> DI void eng_unit(Eng<U>& c) {
  constexpr int C = N % 9, ks = I / U, tile = I % U, B = (C * 2 * U + I) % (PF + 1);
  if constexpr (tile == U - PF && 2 * N + ks + 1 <= 53) {
    wait_vm<younger<U>(1, 2 * N + ks + 1, N, I)>();
    bar();
  }
  if constexpr (N + 1 <= 8 && xrd_at<U>(I) >= 0) {
    constexpr int M = N + 1 <= 8 ? N + 1 : 0, R = xrd_at<U>(I) >= 0 ? xrd_at<U>(I) : 0;
    if constexpr (R == 0) wait_vm<younger<U>(2, M, N, I)>();
    eng_xrd<U, M, R>(c);
    SB();
  }
  bf16x8 w = cat(c.wf[B][0], c.wf[B][1]);
  if constexpr (C == 0 && ks == 0) mfma_zero(c.acc[0][tile], w, c.x[C][0 + ks]); else mfma_acc(c.acc[0][tile], w, c.x[C][0 + ks]);
  SB(); eng_pref<U, N, I, 0>(c); SB();
  if constexpr (C == 0 && ks == 0) mfma_zero(c.acc[1][tile], w, c.x[C][2 + ks]); else mfma_acc(c.acc[1][tile], w, c.x[C][2 + ks]);
  SB(); eng_pref<U, N, I, 1>(c); SB();
  eng_event<U, N, I>(c); SB();
}
template <int U, int N, int... Is> DI void eng_units(Eng<U>& c, std::integer_sequence<int, Is...>) { (eng_unit<U, N, Is>(c), ...); }
template <int U, int N> DI void eng_chunk(Eng<U>& c) {
  if constexpr (U == 8 && N % 3 == 0) __builtin_amdgcn_s_setprio((N % 9) / 3);
  eng_units<U, N>(c, std::make_integer_sequence<int, 2 * U>{});
}
template <int U, int P> DI void eng_pass(Eng<U>& c) {
  STAMP(1 + P * 4);
  eng_chunk<U, 9 * P + 0>(c); eng_chunk<U, 9 * P + 1>(c); eng_chunk<U, 9 * P + 2>(c);
  STAMP(2 + P * 4);
  eng_chunk<U, 9 * P + 3>(c); eng_chunk<U, 9 * P + 4>(c); eng_chunk<U, 9 * P + 5>(c);
  eng_chunk<U, 9 * P + 6>(c); eng_chunk<U, 9 * P + 7>(c); eng_chunk<U, 9 * P + 8>(c);
  if constexpr (U == 8) __builtin_amdgcn_s_setprio(3);
  STAMP(3 + P * 4);
}
template <int U, int... Is> DI void eng_prologue_(Eng<U>& c, std::integer_sequence<int, Is...>) { (eng_event<U, -1, Is>(c), ...); }
template <int U> DI void eng_start(Eng<U>& c, const void* x, char* ws, LAS char* lds, int mt, int lane, int wid) {
  c.lane = lane;
  c.y.rf = mk_rsrc(ws + WS_F, F_BYTES);
  u32x2 seed;
  ld8_sc1(seed, 0u * lane, c.y.rf, F_EP);
  c.rx = mk_rsrc(x, 2048 * 1152);
  {
    int o = lane >> 3, swz = ((o >> 1) & 1) | ((wid & 1) << 1);
    c.wvoff = (unsigned)(16 * wid + o) * ROWB(U) + (unsigned)(((lane & 7) ^ (swz << 1)) * 16);
    int f = (o >> 1) & 1;
    unsigned rowb = (unsigned)((mt * 128 + wid * 32 + o) * 1152);
    c.xvoff[0] = rowb + (unsigned)(((lane & 7) ^ f) * 16);
    c.xvoff[1] = rowb + (unsigned)(((lane & 7) ^ f ^ 6) * 16);
  }
  c.m0w = (unsigned)(size_t)lds + (unsigned)wid * 1024u;
  c.m0x = (unsigned)(size_t)lds + (unsigned)(XOFF + wid * 8192);
  {
    LAS char* xb = lds + XOFF + wid * 8192;
    int g = lane >> 4, t16 = lane & 15, f = ((t16 >> 1) & 1) | ((t16 & 8) ? 6 : 0);
    c.xr[0] = xb + t16 * 128 + (((2 * g) ^ f) * 16);
    c.xr[1] = xb + t16 * 128 + (((2 * g + 1) ^ f) * 16);
  }
  eng_prologue_<U>(c, std::make_integer_sequence<int, npos<U>(-1)>{});
  wait_vm<younger<U>(2, 0, 0, 0)>();
  pin(seed); make_tag(c.y, seed);
  eng_xrd<U, 0, 0>(c); eng_xrd<U, 0, 1>(c); eng_xrd<U, 0, 2>(c); eng_xrd<U, 0, 3>(c);
  bar();
  if (wid == 0) publish(c.y, F_ST + blockIdx.x * 8u, lane);
  eng_prime<U, 0>(c);
}

constexpr int BLK = 18432, NB = 21;
constexpr int PRO2 = 12 + 12;
constexpr Ev ev2_at(int B, int I) {
  if (B < 0) return I < 12 ? Ev{1, I / 5, I % 5} : Ev{2, (I - 12) / 4, (I - 12) % 4};
  if ((I == 14 || I == 16) && B + 3 < NB) return Ev{1, B + 3, (I - 14) / 2};
  if ((I == 0 || I == 2 || I == 4) && B + 2 < NB) return Ev{1, B + 2, 2 + I / 2};
  if ((I == 6 || I == 8 || I == 10 || I == 12) && B + 3 < NB && !(B + 3 == NB - 1 && I >= 10)) return Ev{2, B + 3, (I - 6) / 2};
  if (I == 1 && (B == 2 || B == 8 || B == 14)) return Ev{4, B, 0};
  if (B == 15 && (I == 7 || I == 9)) return Ev{7, 0, (I - 7) / 2};
  return Ev{0, 0, 0};
}
constexpr int npos2(int B) { return B < 0 ? PRO2 : (B == NB - 1 ? 9 : 18); }
constexpr int younger2(int kind, int id, int piece, int B, int I) {
  int cnt = 0; bool seen = false;
  for (int n = -1; n <= B; ++n)
    for (int i = 0; i < (n == B ? I : npos2(n)); ++i) {
      Ev e = ev2_at(n, i);
      if (e.kind == kind && e.id == id && e.piece == piece) { cnt = 0; seen = true; } else if (e.kind != 0) ++cnt;
    }
  return seen ? cnt : -1;
}
struct G2 {
  int stv;
  Sync y;
  rsrc_t rd, ro, rh, rg;
  unsigned l16, wv[5], m0w, wso, aso, hso, oso, goff, fvo, fso;
  int rot, lane, wid;
  LAS char* rb[2];
  u32x4 h[4][4];
  u32x2 fl;
  unsigned gt[2];
  f32x4 acc[2][9];
  bf16x4 wf[PF + 1][2];
};
DI unsigned jj_of(int rot, int j) { const int k = j + rot; return (unsigned)(k >= 6 ? k - 6 : k); }
template <int B, int I> DI void g2_event(G2& c) {
  constexpr Ev e = ev2_at(B, I);
  if constexpr (e.kind == 1) {
    const unsigned m0v = c.m0w + (unsigned)((e.id % 4) * BLK + e.piece * 1024);
    unsigned vo = c.wv[e.piece], so;
    if constexpr (e.id < 18) so = c.wso + jj_of(c.rot, e.id % 6) * (192u * 1152u) + (unsigned)((e.id / 6) * 64 * 1152);
    else if constexpr (e.id < 20) so = c.aso + (unsigned)((e.id - 18) * 72 * 1152) + (unsigned)c.wid * (18u * 1152u);
    else {
      const int n = 64 * e.piece + c.lane, r = n / 18;
      vo += (unsigned)((r >> 2) * 14 * 1152);
      so = c.wid < 2 ? c.aso + (unsigned)(16 * 1152) + (unsigned)c.wid * (72u * 1152u) : OOB;
    }
    if constexpr (e.piece < 4) dma16(m0v, vo, e.id < 18 ? c.rd : c.ro, so); else dma16_half(m0v, vo, e.id < 18 ? c.rd : c.ro, so);
  } else if constexpr (e.kind == 2) {
    unsigned so;
    if constexpr (e.id < 18) so = c.hso + jj_of(c.rot, e.id % 6) * 6144u + (unsigned)((2 * (e.id / 6) + (e.piece >> 1)) * 1024 + (e.piece & 1) * 144 * 1024);
    else so = c.oso + (unsigned)((2 * (e.id - 18) + (e.piece >> 1)) * 1024 + (e.piece & 1) * 20 * 1024);
    ld16_sc1(c.h[e.id % 4][e.piece], c.l16, c.rh, so);
  } else if constexpr (e.kind == 4) {
    ld8_sc1(c.fl, c.fvo, c.y.rf, c.fso);
  } else if constexpr (e.kind == 7) {
    ld4_sc1(c.gt[e.piece], c.goff, c.rg, (unsigned)(e.piece * 64));
  }
}
template <int B, int I, int HALF> DI bf16x4 g2_rd(G2& c) {
  constexpr int o = (B % 4) * BLK + (I / 9) * 9216 + HALF * 4608 + (I % 9) * 32;
  return trd(c.rb[o / 36864] + o % 36864);
}
template <int B, int I, int HALF> DI void g2_pref(G2& c) {
  constexpr int W = (B * 18 + I + PF) % (PF + 1);
  if constexpr (I + PF < npos2(B)) c.wf[W][HALF] = g2_rd<B, I + PF, HALF>(c);
  else if constexpr (B < NB - 1) c.wf[W][HALF] = g2_rd<B + 1, I + PF - 18, HALF>(c);
}
template <int B, int I> DI void g2_unit(G2& c) {
  constexpr int ks = I / 9, u = I % 9, W = (B * 18 + I) % (PF + 1);
  if constexpr (u == 0) {
    wait_vm<younger2(2, B, 2 * ks + 1, B, I)>();
    pin(c.h[B % 4][2 * ks]); pin(c.h[B % 4][2 * ks + 1]);
  }
  if constexpr (I == 5 && (B == 3 || B == 9 || B == 15)) {
    wait_vm<younger2(4, B - 1, 0, B, I)>();
    pin(c.fl);
    constexpr int lo = (B + 3), hi = B == 15 ? 20 : lo + 6;
    poll(c.y, c.fl, c.lane >= lo && c.lane < hi, c.fvo, c.fso);
    SB();
  }
  if constexpr (B == 18 && I == 0) {
    static_assert(younger2(7, 0, 1, B, I) >= younger2(2, B, 1, B, I) - 8, "");
    wait_vm<younger2(7, 0, 1, B, I)>();
    pin(c.gt[0]); pin(c.gt[1]);
    mfma_fence();
#pragma unroll
    for (int tt = 0; tt < 2; ++tt) {
      const float og = 1.0f - __builtin_bit_cast(float, c.gt[tt]);
#pragma unroll
      for (int k = 0; k < 9; ++k) c.acc[tt][k] *= og;
    }
    mfma_fence();
  }
  if constexpr (I == 18 - PF && B < NB - 1) { wait_vm<younger2(1, B + 1, 4, B, I)>(); bar(); }
  bf16x8 w = cat(c.wf[W][0], c.wf[W][1]);
  const u32x4 b0 = c.h[B % 4][2 * ks], b1 = c.h[B % 4][2 * ks + 1];
  if constexpr (B == 0 && ks == 0) mfma_zero(c.acc[0][u], w, b0); else mfma_acc(c.acc[0][u], w, b0);
  SB(); g2_pref<B, I, 0>(c); SB();
  if constexpr (B == 0 && ks == 0) mfma_zero(c.acc[1][u], w, b1); else mfma_acc(c.acc[1][u], w, b1);
  SB(); g2_pref<B, I, 1>(c); SB();
  g2_event<B, I>(c); SB();
}
template <int B, int... Is> DI void g2_units(G2& c, std::integer_sequence<int, Is...>) { (g2_unit<B, Is>(c), ...); }
template <int B> DI void g2_block(G2& c) { g2_units<B>(c, std::make_integer_sequence<int, npos2(B)>{}); }
template <int O, int... Is> DI void g2_pro(G2& c, std::integer_sequence<int, Is...>) { (g2_event<-1, O + Is>(c), ...); }
template <int... Is> DI void g2_prime(G2& c, std::integer_sequence<int, Is...>) { ((c.wf[Is >> 1][Is & 1] = g2_rd<0, (Is >> 1), (Is & 1)>(c)), ...); }
DI f32x4 up4(unsigned a, unsigned b) {
  return f32x4{__builtin_bit_cast(float, a << 16), __builtin_bit_cast(float, a & 0xffff0000u), __builtin_bit_cast(float, b << 16), __builtin_bit_cast(float, b & 0xffff0000u)};
}
DI void phase2(const Sync& y, int stv, unsigned own_flag, const void* wo, const void* wd, char* ws, void* out, LAS char* lds, int xcd, int lo) {
  const int lane = threadIdx.x & 63, wid = rfl(threadIdx.x >> 6);
  const int s = xcd >> 1, hm = xcd & 1, m = hm * 8 + (lo >> 2), ng = lo & 3;
  const int tq = wid, t16 = lane & 15, quad = lane >> 4, tile = m * 4 + ng;
  G2 c;
  c.stv = stv; c.y = y; c.lane = lane; c.wid = wid;
  c.rd = mk_rsrc(wd, 4608 * 1152); c.ro = mk_rsrc(wo, 576 * 1152);
  c.rh = mk_rsrc(ws + WS_H, (unsigned)(H_BYTES + OB_BYTES)); c.rg = mk_rsrc(ws + WS_G, (unsigned)G_BYTES);
  c.l16 = (unsigned)lane * 16u;
#pragma unroll
  for (int i = 0; i < 5; ++i) { const int n = 64 * i + lane, r = n / 18; c.wv[i] = (unsigned)(r * 1152 + (n - 18 * r) * 16); }
  c.m0w = (unsigned)(size_t)lds + (unsigned)wid * 4608u;
  c.wso = (unsigned)((1152 * s + 16 * wid) * 1152 + 288 * ng);
  c.aso = (unsigned)(144 * s * 1152 + 288 * ng);
  c.hso = (unsigned)(((m * 8 + tq * 2) * 144 + 36 * s) * 1024);
  c.oso = (unsigned)H_BYTES + (unsigned)(((m * 8 + tq * 2) * 20 + 5 * s) * 1024);
  c.goff = (unsigned)((s * 2048 + m * 128 + tq * 32 + t16) * 4);
  c.fvo = lane < 20 ? (unsigned)lane * 8u : OOB;
  c.fso = F_H + (unsigned)(((m * 4 + tq) * 4 + s) * 256);
  c.rot = ((lo >> 2) + (3 * ng) / 2) % 6;
  {
    const int q = (lane >> 2) & 3, p = lane & 3;
    c.rb[0] = lds + (4 * quad + q) * 288 + 8 * p; c.rb[1] = c.rb[0] + 36864;
    asm volatile("" : "+v"(c.rb[0]), "+v"(c.rb[1]));
  }
  __builtin_amdgcn_s_setprio(P2_PRIO);
  asm volatile("s_waitcnt lgkmcnt(0)" ::: "memory");
  bar();
  ld8_sc1(c.fl, c.fvo, c.y.rf, c.fso);
  g2_pro<0>(c, std::make_integer_sequence<int, 12>{});
  wait_vm<12>();
  publish(c.y, own_flag, lane);
  pin(c.fl);
  poll(c.y, c.fl, lane < 6, c.fvo, c.fso);
  g2_pro<12>(c, std::make_integer_sequence<int, 12>{});
  wait_vm<younger2(1, 0, 4, 0, 0)>();
  bar();
  g2_prime(c, std::make_integer_sequence<int, 2 * PF>{});
  SB();
  STAMP(16);
  g2_block<0>(c); g2_block<1>(c); g2_block<2>(c); g2_block<3>(c); g2_block<4>(c); g2_block<5>(c);
  STAMP(17);
  g2_block<6>(c); g2_block<7>(c); g2_block<8>(c);
  STAMP(18);
  g2_block<9>(c); g2_block<10>(c); g2_block<11>(c);
  STAMP(19);
  g2_block<12>(c); g2_block<13>(c); g2_block<14>(c);
  STAMP(20);
  g2_block<15>(c); g2_block<16>(c); g2_block<17>(c);
  STAMP(21);
  g2_block<18>(c); g2_block<19>(c); g2_block<20>(c);
  mfma_fence();
  STAMP(22);
  rsrc_t rxc = mk_rsrc(ws + WS_X, (unsigned)X_BYTES);
  const unsigned xo = (unsigned)(tile * 8 + tq * 2) * XUNIT;
  const bool own8 = tq == s;
  f32x4 r0[2], r1[2], r8[2];
#pragma unroll
  for (int tt = 0; tt < 2; ++tt) {
#pragma unroll
    for (int P = 0; P < 4; ++P) {
      const f32x4 p0 = c.acc[tt][2 * P], p1 = c.acc[tt][2 * P + 1];
      const u32x4 o = {pk2(p0[0], p0[1]), pk2(p0[2], p0[3]), pk2(p1[0], p1[1]), pk2(p1[2], p1[3])};
      st16(o, rxc, c.l16, P == s ? OOB : xo + (unsigned)(tt * XUNIT + (P * 3 + s - (s > P ? 1 : 0)) * 1024), 16);
    }
    const u32x2 o = {pk2(c.acc[tt][8][0], c.acc[tt][8][1]), pk2(c.acc[tt][8][2], c.acc[tt][8][3])};
    __builtin_amdgcn_raw_buffer_store_b64(o, rxc, (unsigned)lane * 8u, own8 ? OOB : xo + (unsigned)(tt * XUNIT + 12288 + (s - (s > tq ? 1 : 0)) * 512), 16);
    r0[tt] = c.acc[tt][0]; r1[tt] = c.acc[tt][1]; r8[tt] = c.acc[tt][8];
#pragma unroll
    for (int P = 1; P < 4; ++P) if (s == P) { r0[tt] = c.acc[tt][2 * P]; r1[tt] = c.acc[tt][2 * P + 1]; }
  }
  wait_vm<0>();
  const unsigned xf = F_X + (unsigned)((tile * 4 + tq) * 64);
  publish(c.y, xf + (unsigned)s * 8u, lane);
  STAMP(23);
  {
    const u32x2 none = {~c.y.t0, 0u};
    unsigned spins = 0;
    u32x2 f = none;
    const unsigned fv = lane < 4 ? (unsigned)lane * 8u : OOB;
    do {
      asm volatile("buffer_load_dwordx2 %0, %1, %2, %3 offen sc1\n\ts_waitcnt vmcnt(0)" : "=&v"(f) : "v"(fv), "s"(c.y.rf), "s"(xf) : "memory");
    } while (flags_missing(c.y, f, lane < 4 && lane != s) && ++spins < (1u << 20));
  }
  STAMP(24);
  {
    u32x4 pv[2][3]; u32x2 p8[2][3];
#pragma unroll
    for (int tt = 0; tt < 2; ++tt)
#pragma unroll
      for (int k = 0; k < 3; ++k) {
        ld16_sc1(pv[tt][k], c.l16, rxc, xo + (unsigned)(tt * XUNIT + (s * 3 + k) * 1024));
        ld8_sc1(p8[tt][k], (unsigned)lane * 8u, rxc, own8 ? xo + (unsigned)(tt * XUNIT + 12288 + k * 512) : OOB);
      }
    wait_vm<0>();
#pragma unroll
    for (int tt = 0; tt < 2; ++tt)
#pragma unroll
      for (int k = 0; k < 3; ++k) {
        pin(pv[tt][k]); pin(p8[tt][k]);
        r0[tt] += up4(pv[tt][k][0], pv[tt][k][1]); r1[tt] += up4(pv[tt][k][2], pv[tt][k][3]); r8[tt] += up4(p8[tt][k][0], p8[tt][k][1]);
      }
  }
#pragma unroll
  for (int tt = 0; tt < 2; ++tt) {
    char* orow = (char*)out + (size_t)(m * 128 + tq * 32 + tt * 16 + t16) * 1152 + ng * 288 + quad * 8;
    *(u32x2*)(orow + s * 64) = u32x2{pk2(r0[tt][0], r0[tt][1]), pk2(r0[tt][2], r0[tt][3])};
    *(u32x2*)(orow + s * 64 + 32) = u32x2{pk2(r1[tt][0], r1[tt][1]), pk2(r1[tt][2], r1[tt][3])};
    if (own8) *(u32x2*)(orow + 256) = u32x2{pk2(r8[tt][0], r8[tt][1]), pk2(r8[tt][2], r8[tt][3])};
  }
  STAMP(25);
}

template <int P, int E> DI void ffn_egroup(Eng<8>& c, rsrc_t rh, unsigned hvoff, unsigned hsoff) {
  constexpr int tt = E >> 1, gi = E & 1;
  if constexpr (E == 3 && P < 2) eng_prime<8, 9 * (P + 1)>(c);
  u32x4 o = gelu_mul8(c.acc[tt][2 * gi], c.acc[tt][2 * gi + 1], c.acc[tt][4 + 2 * gi], c.acc[tt][4 + 2 * gi + 1]);
  st16(o, rh, hvoff, hsoff + (unsigned)(P * 2048 + tt * 144 * 1024 + gi * 1024), 16);
  SB();
}
template <int P> DI void ffn_pass(Eng<8>& c, rsrc_t rh, unsigned hvoff, unsigned hsoff) {
  eng_pass<8, P>(c);
  ffn_egroup<P, 0>(c, rh, hvoff, hsoff); ffn_egroup<P, 1>(c, rh, hvoff, hsoff);
  ffn_egroup<P, 2>(c, rh, hvoff, hsoff); ffn_egroup<P, 3>(c, rh, hvoff, hsoff);
  STAMP(4 + P * 4);
}
DI void k1_ffn(Sync& y, int& stv, unsigned& own, const void* x, const void* wg, const void* wu, char* ws, LAS char* lds, int xcd, int idx) {
  const int lane = threadIdx.x & 63, wid = rfl(threadIdx.x >> 6);
  const int s = xcd >> 1, hm = xcd & 1, jj = idx % 6, ml = idx / 6, mt = hm * 8 + ml, j = 6 * s + jj;
  Eng<8> c;
  c.r0 = mk_rsrc(wg, 576 * 9216); c.r1 = mk_rsrc(wu, 576 * 9216);
  rsrc_t rh = mk_rsrc(ws + WS_H, (unsigned)H_BYTES);
  const unsigned hvoff = (unsigned)lane * 16u, hsoff = (unsigned)(((mt * 8 + wid * 2) * 144 + j * 6) * 1024);
  c.colb = (unsigned)j * 384u;
  c.foff = F_H + (unsigned)(((mt * 4 + wid) * 4 + s) * 256 + jj * 8);
  {
    int g = lane >> 4, q = (lane >> 2) & 3, p = lane & 3, swz = (q >> 1) | ((g & 1) << 1);
#pragma unroll
    for (int i = 0; i < 4; ++i) { c.rb[i] = lds + 1024 * g + 128 * q + 8 * p + ((i ^ swz) * 32); asm volatile("" : "+v"(c.rb[i])); }
  }
  c.stv = 0;
  STAMP(0); RSTAMP(13);
  c.rot = ((0x7420 >> (4 * (ml & 3)) & 15) + (0x865320 >> (4 * jj) & 15)) % 9;
  if (ml >= 4) __builtin_amdgcn_s_sleep(S2_SLEEP);
  eng_start<8>(c, x, ws, lds, mt, lane, wid);
  ffn_pass<0>(c, rh, hvoff, hsoff); ffn_pass<1>(c, rh, hvoff, hsoff); ffn_pass<2>(c, rh, hvoff, hsoff);
  RSTAMP(14);
  y = c.y; stv = c.stv; own = c.foff + 96u;
}

constexpr int KVP = 144;
template <int P> DI void aux_pack(Eng<5>& c, u32x2 (&o)[2][5]) {
  mfma_fence();
  if constexpr (P == 0) eng_prime<5, 9 * (P + 1)>(c);
#pragma unroll
  for (int tt = 0; tt < 2; ++tt)
#pragma unroll
    for (int t = 0; t < 5; ++t) {
      const f32x4 v = c.acc[tt][t];
      if constexpr (P != 1) o[tt][t] = u32x2{pk2(phi(v[0]), phi(v[1])), pk2(phi(v[2]), phi(v[3]))};
      else o[tt][t] = u32x2{pk2(v[0], v[1]), pk2(v[2], v[3])};
      pin(o[tt][t]);
    }
  mfma_fence();
}
DI void k1_aux(Sync& y, int& stv, unsigned& own, const void* x, const void* wqkv, const void* gw, char* ws, LAS char* lds, int xcd, int a) {
  const int lane = threadIdx.x & 63, wid = rfl(threadIdx.x >> 6);
  const int s = xcd >> 1, hm = xcd & 1, mt = hm * 8 + (a & 7), hp = a >> 3, hg = 2 * s + hp;
  const int g = lane >> 4, q = (lane >> 2) & 3, p = lane & 3, t16 = lane & 15;
  const int h = 4 * hg + wid, c0 = (18 * wid) & ~3, off = (18 * wid) & 3;
  Eng<5> c;
  c.r0 = mk_rsrc(wqkv, 576 * 3456); c.r1 = c.r0;
  c.rot = (a & 7) + hp;
  c.colb = (unsigned)hg * 128u;
  c.foff = F_K + (unsigned)(h * 128 + mt * 8);
  {
    int swz = (q >> 1) | ((g & 1) << 1);
#pragma unroll
    for (int t = 0; t < 5; ++t) {
      int b = 16 * hg + 32 * t + 8 * p; b = b > 248 ? 248 : b;
      c.rb[t] = lds + (b >> 7) * 4096 + 1024 * g + 128 * q + ((((b >> 5) & 3) ^ swz) * 32) + (b & 31);
      asm volatile("" : "+v"(c.rb[t]));
    }
  }
  c.stv = 0; c.dummy = 0;
  STAMP(0); RSTAMP(13);
  {
    rsrc_t rx = mk_rsrc(x, 2048 * 1152);
    const unsigned v = lane < 36 ? (unsigned)(((hm * 8 + 4) * 128 + a * 32 + wid * 8) * 1152 + lane * 128) : OOB;
    touch(c.dummy, v, rx, 0u); touch(c.dummy, v, rx, 4608u);
    wait_vm<0>(); pin(c.dummy);
  }
  eng_start<5>(c, x, ws, lds, mt, lane, wid);
  rsrc_t rkv = mk_rsrc(ws + WS_KV, (unsigned)KV_BYTES);
  u32x2 pk[2][5], pv[2][5];
  eng_pass<5, 0>(c);
  {
    rsrc_t rgw = mk_rsrc(gw, 1152);
    const unsigned m0g = (unsigned)(size_t)lds + (unsigned)(XOFF + 24576 + wid * 2048);
    dma16(m0g, (unsigned)lane * 16u, rgw, 0u); dma16(m0g + 1024u, (unsigned)lane * 16u, rgw, 1024u);
  }
  aux_pack<0>(c, pk); STAMP(4);
  eng_pass<5, 1>(c); aux_pack<1>(c, pv);
  {
    LAS char* imgK = lds + XOFF; LAS char* imgV = lds + XOFF + 10240;
    f32x4 kv[2][2] = {}, z[2] = {};
    const u32x4 one4 = {0x3f803f80u, 0x3f803f80u, 0x3f803f80u, 0x3f803f80u};
    const bf16x8 ones = __builtin_bit_cast(bf16x8, one4);
    const int wo_ = (wid * 16 + t16) * KVP + g * 8, ro = (8 * g + q) * KVP + c0 * 2 + p * 8;
#pragma unroll
    for (int tt = 0; tt < 2; ++tt) {
#pragma unroll
      for (int t = 0; t < 5; ++t)
        if (t < 4 || g < 2) { *(LAS u32x2*)(imgK + wo_ + t * 32) = pk[tt][t]; *(LAS u32x2*)(imgV + wo_ + t * 32) = pv[tt][t]; }
      asm volatile("s_waitcnt lgkmcnt(0)" ::: "memory");
      bar();
#pragma unroll
      for (int ks = 0; ks < 2; ++ks) {
        bf16x8 fa[2], fb[2];
#pragma unroll
        for (int i = 0; i < 2; ++i) {
          fa[i] = cat(trd(imgK + ro + ks * 32 * KVP + i * 32), trd(imgK + ro + ks * 32 * KVP + i * 32 + 4 * KVP));
          fb[i] = cat(trd(imgV + ro + ks * 32 * KVP + i * 32), trd(imgV + ro + ks * 32 * KVP + i * 32 + 4 * KVP));
        }
#pragma unroll
        for (int i = 0; i < 2; ++i) {
          kv[i][0] = mfma(fa[i], fb[0], kv[i][0]); kv[i][1] = mfma(fa[i], fb[1], kv[i][1]); z[i] = mfma(fa[i], ones, z[i]);
        }
        SB();
      }
      asm volatile("s_waitcnt lgkmcnt(0)" ::: "memory");
      bar();
    }
    const unsigned so = (unsigned)((h * 16 + mt) * 2048);
#pragma unroll
    for (int i = 0; i < 2; ++i) {
      const bool dok = i == 0 || g == 0;
#pragma unroll
      for (int jb = 0; jb < 2; ++jb) {
        const int e = 16 * jb + t16 - off;
        const bool ok = dok && e >= 0 && e < 18;
        st16(__builtin_bit_cast(u32x4, kv[i][jb]), rkv, ok ? (unsigned)((e * 24 + 16 * i + 4 * g) * 4) : OOB, so, 16);
      }
      st16(__builtin_bit_cast(u32x4, z[i]), rkv, (dok && t16 == 0) ? (unsigned)((18 * 24 + 16 * i + 4 * g) * 4) : OOB, so, 16);
    }
    mfma_fence();
  }
  float gate[2];
  {
    f32x4 l0 = {0.f, 0.f, 0.f, 0.f}, l1 = {0.f, 0.f, 0.f, 0.f};
    LAS char* gl = lds + XOFF + 24576 + wid * 2048 + g * 32;
#pragma unroll
    for (int i = 0; i < 18; ++i) {
      const bf16x8 w = *(LAS bf16x8*)(gl + kc_of(c.rot, i >> 1) * 128 + (i & 1) * 16);
      l0 = mfma(w, __builtin_bit_cast(bf16x8, c.x[i >> 1][i & 1]), l0);
      l1 = mfma(w, __builtin_bit_cast(bf16x8, c.x[i >> 1][2 + (i & 1)]), l1);
      if (i % 6 == 5) SB();
    }
    gate[0] = 1.0f / (1.0f + __expf(-l0[0])); gate[1] = 1.0f / (1.0f + __expf(-l1[0]));
    asm volatile("" : "+v"(gate[0]), "+v"(gate[1]));
    mfma_fence();
  }
  eng_prime<5, 18>(c);
  STAMP(8);
  eng_pass<5, 2>(c);
  __builtin_amdgcn_s_setprio(2);
  u32x2 pq[2][5];
  LAS char* ka = lds + XOFF;
  {
    const u32x2 none = {~c.y.t0, 0u};
    poll(c.y, none, lane < 16, lane < 16 ? (unsigned)lane * 8u : OOB, F_K + (unsigned)h * 128u);
    f32x4 sa = {0.f, 0.f, 0.f, 0.f}, sb = {0.f, 0.f, 0.f, 0.f};
    const unsigned v0 = (unsigned)lane * 16u, v1 = lane < 50 ? v0 + 1024u : OOB;
    u32x4 la[16], lb[16];
#pragma unroll
    for (int i = 0; i < 16; ++i) { ld16_sc1(la[i], v0, rkv, (unsigned)((h * 16 + i) * 2048)); ld16_sc1(lb[i], v1, rkv, (unsigned)((h * 16 + i) * 2048)); }
    aux_pack<2>(c, pq);
    wait_vm<0>();
#pragma unroll
    for (int i = 0; i < 16; ++i) { pin(la[i]); pin(lb[i]); sa += __builtin_bit_cast(f32x4, la[i]); sb += __builtin_bit_cast(f32x4, lb[i]); }
    const int da = 4 * (lane % 6) - off, db = 4 * ((lane + 64) % 6) - off;
#pragma unroll
    for (int j = 0; j < 4; ++j) {
      sa[j] = (da + j >= 0 && da + j < 18) ? sa[j] : 0.f;
      sb[j] = (db + j >= 0 && db + j < 18 && lane < 50) ? sb[j] : 0.f;
    }
    *(LAS u32x2*)(ka + wid * 1024 + lane * 8) = u32x2{pk2(sa[0], sa[1]), pk2(sa[2], sa[3])};
    *(LAS u32x2*)(ka + wid * 1024 + 512 + lane * 8) = u32x2{pk2(sb[0], sb[1]), pk2(sb[2], sb[3])};
  }
  asm volatile("s_waitcnt lgkmcnt(0)" ::: "memory");
  bar();
  LAS char* iq = lds + wid * 5120;
#pragma unroll
  for (int tt = 0; tt < 2; ++tt)
#pragma unroll
    for (int t = 0; t < 5; ++t)
      if (t < 4 || g < 2) *(LAS u32x2*)(iq + (tt * 16 + t16) * KVP + t * 32 + g * 8) = pq[tt][t];
  rsrc_t rob = mk_rsrc(ws + WS_OB, (unsigned)OB_BYTES);
#pragma unroll
  for (int tt = 0; tt < 2; ++tt) {
    u32x4 ob[2]; float l0 = 0.f, l1 = 0.f;
#pragma unroll
    for (int hh = 0; hh < 4; ++hh) {
      constexpr int w0[4] = {0, 4, 9, 13};
      const int ca = w0[hh] + (g < 3 ? 2 * g : 0), cb = w0[hh] + (g < 2 ? 2 * g + 1 : 0);
      u32x2 ba = *(LAS u32x2*)(iq + (tt * 16 + t16) * KVP + ca * 8), bb = *(LAS u32x2*)(iq + (tt * 16 + t16) * KVP + cb * 8);
      const u32x4 bq = {ba[0], ba[1], bb[0], bb[1]};
      const int e1 = (t16 & 3) == 2 ? 18 : (((t16 >> 2) == hh && (t16 & 3) < 2) ? 16 + (t16 & 3) : 19);
      const bf16x8 a0 = *(LAS bf16x8*)(ka + hh * 1024 + (g < 3 ? t16 * 48 + g * 16 : 19 * 48));
      const bf16x8 a1 = *(LAS bf16x8*)(ka + hh * 1024 + (g < 3 ? e1 * 48 + g * 16 : 19 * 48));
      const f32x4 z = {0.f, 0.f, 0.f, 0.f};
      const f32x4 n1 = mfma(a0, __builtin_bit_cast(bf16x8, bq), z), n2 = mfma(a1, __builtin_bit_cast(bf16x8, bq), z);
      const float sc = gate[tt] * __builtin_amdgcn_rcpf(n2[2] + 1e-6f);
      ob[hh >> 1][2 * (hh & 1)] = pk2(n1[0] * sc, n1[1] * sc);
      ob[hh >> 1][2 * (hh & 1) + 1] = pk2(n1[2] * sc, n1[3] * sc);
      l0 += n2[0] * sc; l1 += n2[1] * sc;
    }
    const unsigned so = (unsigned)(((mt * 8 + wid * 2 + tt) * 20 + 5 * s) * 1024);
    st16(ob[0], rob, (unsigned)lane * 16u, so + (unsigned)(2 * hp) * 1024u, 16);
    st16(ob[1], rob, (unsigned)lane * 16u, so + (unsigned)(2 * hp + 1) * 1024u, 16);
    __builtin_amdgcn_raw_buffer_store_b64(u32x2{pk2(l0, l1), 0u}, rob, (unsigned)lane * 16u + (unsigned)hp * 8u, so + 4096u, 16);
  }
  {
    rsrc_t rg = mk_rsrc(ws + WS_G, (unsigned)G_BYTES);
    const float gl = (lane & 16) ? gate[1] : gate[0];
    __builtin_amdgcn_raw_buffer_store_b32(__builtin_bit_cast(unsigned, gl), rg, (hp == 0 && lane < 32) ? (unsigned)lane * 4u : OOB,
                                          (unsigned)((s * 2048 + mt * 128 + wid * 32) * 4), 16);
  }
  STAMP(12); RSTAMP(14);
  y = c.y; stv = c.stv; own = F_H + (unsigned)(((mt * 4 + wid) * 4 + s) * 256 + (18 + hp) * 8);
}
#endif

__global__ __launch_bounds__(256, 2) void fused(const void* x, const void* wqkv, const void* wo, const void* wg, const void* wu,
                                                const void* wd, const void* gw, char* ws, void* out) {
#if defined(__HIP_DEVICE_COMPILE__)
  extern __shared__ __attribute__((aligned(16))) char smem[];
  LAS char* lds = (LAS char*)smem;
  const int xcd = blockIdx.x & 7, idx = blockIdx.x >> 3, lo = idx & 31, hi = idx >> 5;
  Sync y; int stv; unsigned own;
  if (lo < 24) k1_ffn(y, stv, own, x, wg, wu, ws, lds, xcd, hi * 24 + lo);
  else k1_aux(y, stv, own, x, wqkv, gw, ws, lds, xcd, hi * 8 + lo - 24);
  if (hi == 0) phase2(y, stv, own, wo, wd, ws, out, lds, xcd, lo);
  else {
    wait_vm<0>();
    publish(y, own, threadIdx.x & 63);
    if (blockIdx.x == 256) {
      const u32x2 none = {~y.t0, 0u};
      poll(y, none, true, threadIdx.x * 8u, F_ST);
      poll(y, none, true, threadIdx.x * 8u, F_ST + 2048u);
      __syncthreads();
      if (threadIdx.x < 64) publish(y, F_EP, threadIdx.x);
    }
  }
#endif
}

extern "C" void kernel_launch(void* const* d_in, const int* in_sizes, int n_in, void* d_out, int out_size, void* d_ws, size_t ws_size,
                              hipStream_t stream) {
  (void)hipFuncSetAttribute((const void*)fused, hipFuncAttributeMaxDynamicSharedMemorySize, K1_LDS);
  fused<<<512, 256, K1_LDS, stream>>>(d_in[0], d_in[1], d_in[2], d_in[4], d_in[5], d_in[6], d_in[7], (char*)d_ws, d_out);
}
```

```cpp
#include <hip/hip_runtime.h>
#include <hip/hip_bf16.h>
#include <utility>

typedef __bf16 bf16x8 __attribute__((ext_vector_type(8)));
typedef __bf16 bf16x4 __attribute__((ext_vector_type(4)));
typedef __bf16 bf16x2 __attribute__((ext_vector_type(2)));
typedef float f32x4 __attribute__((ext_vector_type(4)));
typedef float f32x2 __attribute__((ext_vector_type(2)));
typedef unsigned u32x4 __attribute__((ext_vector_type(4)));
typedef unsigned u32x2 __attribute__((ext_vector_type(2)));
#define LAS __attribute__((address_space(3)))
#define DI __device__ __forceinline__
#define RSRC3 0x00020000

constexpr size_t WS_H = 0;
constexpr size_t H_BYTES = (size_t)128 * 144 * 1024;
constexpr size_t WS_OB = WS_H + H_BYTES;
constexpr size_t OB_BYTES = (size_t)128 * 20 * 1024;
constexpr size_t WS_KV = WS_OB + OB_BYTES;
constexpr size_t KV_BYTES = (size_t)32 * 16 * 2048;
constexpr size_t WS_G = WS_KV + KV_BYTES;
constexpr size_t G_BYTES = 4 * 2048 * 4;
constexpr size_t WS_X = WS_G + G_BYTES;
constexpr unsigned XUNIT = 13824;
constexpr size_t X_BYTES = (size_t)512 * XUNIT;
constexpr size_t WS_F = WS_X + X_BYTES;
constexpr unsigned F_EP = 0;
constexpr unsigned F_ST = 4096;
constexpr unsigned F_H = F_ST + 512 * 8;
constexpr unsigned F_K = F_H + 16 * 4 * 4 * 256;
constexpr unsigned F_X = F_K + 32 * 128;
constexpr unsigned F_BYTES = F_X + 64 * 4 * 64;
constexpr int K1_LDS = 81920;

#define AUX_PRIO 1
#define P2_PRIO 2
#define S2_SLEEP 70
#define SPIN_MAX (1u << 16)
#define OWN_B 1
#define P2_M(lo) ((lo) >> 2)
#define ATT_LAST 1
#define TOUCH_P 1

#if defined(__HIP_DEVICE_COMPILE__)
typedef __amdgpu_buffer_rsrc_t rsrc_t;
constexpr unsigned OOB = 0x7ff00000u;
DI rsrc_t mk_rsrc(const void* p, unsigned bytes) { return __builtin_amdgcn_make_buffer_rsrc((void*)p, (short)0, (int)bytes, RSRC3); }
DI unsigned rfl(unsigned x) { return __builtin_amdgcn_readfirstlane(x); }
template <int N> DI void wait_vm() { static_assert(N >= 0 && N < 64, "vmcnt"); asm volatile("s_waitcnt vmcnt(%0)" ::"i"(N) : "memory"); }
DI void bar() { __builtin_amdgcn_s_barrier(); }
#define SB() __builtin_amdgcn_sched_barrier(0)
DI void dma16(unsigned m0v, unsigned voff, rsrc_t rs, unsigned soff) {
  asm volatile("s_mov_b32 m0, %0\n\ts_nop 0\n\tbuffer_load_dwordx4 %1, %2, %3 offen lds" ::"s"(m0v), "v"(voff), "s"(rs), "s"(soff) : "memory");
}
template <int STEP> DI void dma16s(unsigned m0v, unsigned voff, rsrc_t rs, unsigned soff) {
  if constexpr (STEP == 0) dma16(m0v, voff, rs, soff);
  else if constexpr (STEP == 1) asm volatile("s_mov_b32 m0, %0" ::"s"(m0v) : "memory");
  else asm volatile("buffer_load_dwordx4 %0, %1, %2 offen lds" ::"v"(voff), "s"(rs), "s"(soff) : "memory");
}
DI void ld16_sc1(u32x4& dst, unsigned voff, rsrc_t rs, unsigned soff) {
  asm volatile("buffer_load_dwordx4 %0, %1, %2, %3 offen sc1" : "=v"(dst) : "v"(voff), "s"(rs), "s"(soff) : "memory");
}
DI void ld4_sc1(unsigned& dst, unsigned voff, rsrc_t rs, unsigned soff) {
  asm volatile("buffer_load_dword %0, %1, %2, %3 offen sc1" : "=v"(dst) : "v"(voff), "s"(rs), "s"(soff) : "memory");
}
DI void touch(unsigned& dst, unsigned voff, rsrc_t rs, unsigned soff) {
  asm volatile("buffer_load_dword %0, %1, %2, %3 offen" : "+v"(dst) : "v"(voff), "s"(rs), "s"(soff) : "memory");
}
DI void dma16_half(unsigned m0v, unsigned voff, rsrc_t rs, unsigned soff) {
  asm volatile("s_mov_b32 m0, %0\n\ts_mov_b32 exec_hi, 0\n\tbuffer_load_dwordx4 %1, %2, %3 offen lds\n\ts_mov_b32 exec_hi, -1"
               ::"s"(m0v), "v"(voff), "s"(rs), "s"(soff) : "memory");
}
DI void st16(u32x4 v, rsrc_t rs, unsigned voff, unsigned soff, int) {
  __builtin_amdgcn_raw_buffer_store_b128(v, rs, voff, soff, 16);
  asm volatile("s_nop 1" ::"v"(v) : "memory");
}
DI void pin(u32x4& a) { asm volatile("" : "+v"(a)); }
DI void pin(unsigned& a) { asm volatile("" : "+v"(a)); }
DI bf16x4 trd(LAS char* p) { return __builtin_amdgcn_ds_read_tr16_b64_v4bf16((LAS bf16x4*)p); }
DI bf16x8 cat(bf16x4 lo, bf16x4 hi) { return __builtin_shufflevector(lo, hi, 0, 1, 2, 3, 4, 5, 6, 7); }
DI f32x4 mfma(bf16x8 a, bf16x8 b, f32x4 c) { return __builtin_amdgcn_mfma_f32_16x16x32_bf16(a, b, c, 0, 0, 0); }
DI void mfma_acc(f32x4& c, bf16x8 a, u32x4 b) { asm volatile("v_mfma_f32_16x16x32_bf16 %0, %1, %2, %0" : "+v"(c) : "v"(a), "v"(b)); }
DI void mfma_zero(f32x4& c, bf16x8 a, u32x4 b) { asm volatile("v_mfma_f32_16x16x32_bf16 %0, %1, %2, 0" : "=&v"(c) : "v"(a), "v"(b)); }
DI void mfma_fence() { SB(); asm volatile("s_nop 7\n\ts_nop 1" ::: "memory"); SB(); }
DI unsigned pk2(float lo, float hi) { f32x2 v = {lo, hi}; return __builtin_bit_cast(unsigned, __builtin_convertvector(v, bf16x2)); }
DI u32x4 gelu_mul8(f32x4 ga, f32x4 gb, f32x4 ua, f32x4 ub) {
  const float c1 = -2.0f * 1.4426950408889634f * 0.7978845608028654f, c2 = c1 * 0.044715f;
  float t0, t1, t2, t3, t4, t5, t6, t7;
#define R8(X) X(8, 0, 16) X(9, 1, 17) X(10, 2, 18) X(11, 3, 19) X(12, 4, 20) X(13, 5, 21) X(14, 6, 22) X(15, 7, 23)
#define S1(g, t, u) "v_mul_f32 %" #t ", %" #g ", %" #g "\n\t"
#define S2(g, t, u) "v_fma_f32 %" #t ", %" #t ", %25, %24\n\t"
#define S3(g, t, u) "v_mul_f32 %" #t ", %" #t ", %" #g "\n\t"
#define S4(g, t, u) "v_exp_f32 %" #t ", %" #t "\n\t"
#define S5(g, t, u) "v_mul_f32 %" #t ", %" #t ", %" #u "\n\t"
#define S6(g, t, u) "v_add_f32 %" #t ", 1.0, %" #t "\n\t"
#define S7(g, t, u) "v_rcp_f32 %" #t ", %" #t "\n\t"
  asm volatile(R8(S1) R8(S2) R8(S3) R8(S4) R8(S6) R8(S7) R8(S3) R8(S5)
               "v_cvt_pk_bf16_f32 %0, %0, %1\n\tv_cvt_pk_bf16_f32 %1, %2, %3\n\tv_cvt_pk_bf16_f32 %2, %4, %5\n\tv_cvt_pk_bf16_f32 %3, %6, %7"
               : "=&v"(t0), "=&v"(t1), "=&v"(t2), "=&v"(t3), "=&v"(t4), "=&v"(t5), "=&v"(t6), "=&v"(t7)
               : "v"(ga[0]), "v"(ga[1]), "v"(ga[2]), "v"(ga[3]), "v"(gb[0]), "v"(gb[1]), "v"(gb[2]), "v"(gb[3]),
                 "v"(ua[0]), "v"(ua[1]), "v"(ua[2]), "v"(ua[3]), "v"(ub[0]), "v"(ub[1]), "v"(ub[2]), "v"(ub[3]), "v"(c1), "s"(c2));
  u32x4 o = {__builtin_bit_cast(unsigned, t0), __builtin_bit_cast(unsigned, t1), __builtin_bit_cast(unsigned, t2), __builtin_bit_cast(unsigned, t3)};
  return o;
}
DI float phi(float t) { return t > 0.f ? t + 1.000001f : __builtin_amdgcn_exp2f(t * 1.4426950408889634f) + 1e-6f; }
struct Sync { rsrc_t rf; unsigned t0, t1; };
DI void ld8_sc1(u32x2& dst, unsigned voff, rsrc_t rs, unsigned soff) {
  asm volatile("buffer_load_dwordx2 %0, %1, %2, %3 offen sc1" : "=v"(dst) : "v"(voff), "s"(rs), "s"(soff) : "memory");
}
DI void pin(u32x2& a) { asm volatile("" : "+v"(a)); }
DI void publish(const Sync& y, unsigned off, int lane) {
  const u32x2 t = {y.t0, y.t1};
  __builtin_amdgcn_raw_buffer_store_b64(t, y.rf, lane == 0 ? 0u : OOB, off, 16);
}
DI bool flags_missing(const Sync& y, u32x2 f, bool want) { return __builtin_amdgcn_ballot_w64(want && (f[0] != y.t0 || f[1] != y.t1)) != 0ull; }
DI void poll(const Sync& y, u32x2 f, bool want, unsigned voff, unsigned soff) {
  unsigned spins = 0;
  while (flags_missing(y, f, want) && ++spins < SPIN_MAX) {
    __builtin_amdgcn_s_sleep(4);
    asm volatile("buffer_load_dwordx2 %0, %1, %2, %3 offen sc1\n\ts_waitcnt vmcnt(0)" : "=&v"(f) : "v"(voff), "s"(y.rf), "s"(soff) : "memory");
  }
}

constexpr int HSLOT = 8192;
constexpr int NH = 6, PF = 4;
constexpr int XOFF = NH * HSLOT;

struct Ev { int kind, id, piece; };
constexpr int PRO = 14, NT0 = 0;
constexpr int slot_of(int U, int h) { return h % NH; }
constexpr int UU(int K) { return K / 10; }
constexpr int NPP(int K) { return K % 10; }
template <int K> constexpr Ev ev_at(int N, int I) {
  constexpr int U = UU(K), HL = 18 * NPP(K) - 1;
  if (N < 0) {
    if (I >= PRO) return Ev{1, 3 + (I - PRO) / 2, (I - PRO) & 1};
    constexpr int k[PRO] = {1, 1, 2, 2, 2, 2, 1, 1, 1, 1, 2, 2, 2, 2}, id[PRO] = {0, 0, 0, 0, 0, 0, 1, 1, 2, 2, 1, 1, 1, 1},
                  pc[PRO] = {0, 1, 0, 1, 2, 3, 0, 1, 0, 1, 0, 1, 2, 3};
    return Ev{k[I], id[I], pc[I]};
  }
  if (N == 0) {
    if (U == 8) {
      if (I < 4) return Ev{1, 3 + I / 2, I & 1};
      if (I == 5 || I == 7 || I == 8 || I == 10) return Ev{2, 2, I == 5 ? 0 : I == 7 ? 1 : I == 8 ? 2 : 3};
    }
  }
  if (I >= 2 * U) return Ev{3, N / 9, 0};
  if (N >= 9 && N % 9 == 3 && I == (U == 8 ? 1 : 0) && (U == 8 || N == 21)) return Ev{9, N / 9 - 1, 0};
  const int ks = I / U, t = I % U;
  constexpr int dd = NH - 1;
  if ((t == U - PF || t == U - PF + 2) && 2 * N + ks + dd <= HL) return Ev{1, 2 * N + ks + dd, (t - (U - PF)) / 2};
  if (N + 2 <= 8) {
    if (U == 8 && I < 4) return Ev{2, N + 2, I};
    if (U == 5 && (I == 0 || I == 2 || I == 4 || I == 5)) return Ev{2, N + 2, I == 5 ? 3 : I / 2};
  }
  return Ev{0, 0, 0};
}
constexpr int eops(int P) { return P == TOUCH_P ? 5 : 4; }
template <int K> constexpr int npos(int N) { constexpr int U = UU(K); return N < 0 ? (U == 8 ? PRO : PRO + 4) : (N % 9 == 8 && U == 8 ? 2 * U + eops(N / 9) : 2 * U); }
template <int K> constexpr int younger(int kind, int id, int N, int I) {
  int cnt = 0; bool seen = false;
  for (int n = -1; n <= N; ++n)
    for (int i = 0; i < (n == N ? I : npos<K>(n)); ++i) {
      Ev e = ev_at<K>(n, i);
      if (e.kind == kind && e.id == id) { cnt = 0; seen = true; } else if (e.kind != 0) ++cnt;
    }
  return seen ? cnt : -1;
}
template <int U> constexpr int xrd_at(int I) {
  if (U == 8) return I == 9 ? 0 : I == 10 ? 1 : I == 11 ? 2 : I == 13 ? 3 : -1;
  return I >= 6 ? I - 6 : -1;
}

template <int U> struct Eng {
  rsrc_t r0, r1, rx;
  unsigned wvoff, xvoff[2];
  unsigned m0w, m0x, colb, dummy, foff;
  int lane;
  Sync y;
  int rot;
  LAS char* rb[U == 8 ? 4 : 5];
  LAS char* xr[2];
  u32x4 x[9][4];
  f32x4 acc[2][U];
  bf16x4 wf[PF + 1][2];
};
constexpr unsigned ROWB(int U) { return U == 8 ? 9216u : 3456u; }
constexpr unsigned PASSB(int U, int P) { return U == 8 ? 128u * P : 1152u * ((P + 1) % 3); }
DI unsigned kc_of(int rot, int C) { const int kc = C + rot; return (unsigned)(kc >= 9 ? kc - 9 : kc); }
template <int K, int N, int I, int STEP = 0> DI void eng_event(Eng<UU(K)>& c) {
  constexpr int U = UU(K);
  constexpr Ev e = ev_at<K>(N, I);
  if constexpr (e.kind == 1) {
    constexpr int n = e.id >> 1, ks = e.id & 1;
    constexpr unsigned so = (unsigned)(ks * 8) * ROWB(U) + PASSB(U, n / 9) + (U == 5 ? e.piece * 128u : 0u);
    dma16s<STEP>(c.m0w + (unsigned)(slot_of(U, e.id) * HSLOT + e.piece * 4096), c.wvoff, e.piece ? c.r1 : c.r0, kc_of(c.rot, n % 9) * (64u * ROWB(U)) + c.colb + so);
  } else if constexpr (e.kind == 2) {
    dma16s<STEP>(c.m0x + (unsigned)((e.id & 1) * 4096 + e.piece * 1024), c.xvoff[e.piece & 1], c.rx, kc_of(c.rot, e.id) * 128u + (unsigned)(e.piece * 8 * 1152));
  } else if constexpr (e.kind == 9 && STEP != 1) {
    if constexpr (U == 8) { wait_vm<younger<K>(3, e.id, N, I)>(); publish(c.y, c.foff + (unsigned)(e.id * 8), c.lane); }
    else { wait_vm<0>(); publish(c.y, c.foff, c.lane); }
  }
}
template <int U, int N, int I, int HALF> DI bf16x4 eng_rd(Eng<U>& c) {
  constexpr int ks = I / U, tile = I % U;
  if constexpr (U == 8) return trd(c.rb[tile & 3] + slot_of(U, 2 * N + ks) * HSLOT + (tile >> 2) * 4096 + HALF * 512);
  else return trd(c.rb[tile] + ((2 * N + ks) % NH) * HSLOT + HALF * 512);
}
template <int U, int N, int I, int HALF> DI void eng_pref(Eng<U>& c) {
  constexpr int B = ((N % 9) * 2 * U + I + PF) % (PF + 1);
  if constexpr (I + PF < 2 * U) c.wf[B][HALF] = eng_rd<U, N, I + PF, HALF>(c);
  else if constexpr (N % 9 < 8) c.wf[B][HALF] = eng_rd<U, N + 1, I + PF - 2 * U, HALF>(c);
}
template <int U, int N, int... Is> DI void eng_prime_(Eng<U>& c, std::integer_sequence<int, Is...>) {
  ((c.wf[(Is >> 1) % (PF + 1)][Is & 1] = eng_rd<U, N, (Is >> 1), (Is & 1)>(c)), ...);
}
template <int U, int N> DI void eng_prime(Eng<U>& c) { eng_prime_<U, N>(c, std::make_integer_sequence<int, 2 * PF>{}); SB(); }
template <int U, int M, int R> DI void eng_xrd(Eng<U>& c) {
  c.x[M][R] = *(LAS u32x4*)(c.xr[R & 1] + (M & 1) * 4096 + (R >> 1) * 2048);
}
template <int K, int N, int I> DI void eng_unit(Eng<UU(K)>& c) {
  constexpr int U = UU(K);
  constexpr int C = N % 9, ks = I / U, tile = I % U, B = (C * 2 * U + I) % (PF + 1);
  if constexpr (tile == U - PF && 2 * N + ks + 1 <= 18 * NPP(K) - 1) {
    wait_vm<younger<K>(1, 2 * N + ks + 1, N, I)>();
    bar();
  }
  if constexpr (N + 1 <= 8 && xrd_at<U>(I) >= 0) {
    constexpr int M = N + 1 <= 8 ? N + 1 : 0, R = xrd_at<U>(I) >= 0 ? xrd_at<U>(I) : 0;
    if constexpr (R == 0) wait_vm<younger<K>(2, M, N, I)>();
    eng_xrd<U, M, R>(c);
    SB();
  }
  bf16x8 w = cat(c.wf[B][0], c.wf[B][1]);
  if constexpr (C == 0 && ks == 0) mfma_zero(c.acc[0][tile], w, c.x[C][0 + ks]); else mfma_acc(c.acc[0][tile], w, c.x[C][0 + ks]);
  SB(); eng_pref<U, N, I, 0>(c); eng_event<K, N, I, 1>(c); SB();
  if constexpr (C == 0 && ks == 0) mfma_zero(c.acc[1][tile], w, c.x[C][2 + ks]); else mfma_acc(c.acc[1][tile], w, c.x[C][2 + ks]);
  SB(); eng_pref<U, N, I, 1>(c); SB();
  eng_event<K, N, I, 2>(c); SB();
}
template <int K, int N, int... Is> DI void eng_units(Eng<UU(K)>& c, std::integer_sequence<int, Is...>) { (eng_unit<K, N, Is>(c), ...); }
template <int K, int N> DI void eng_chunk(Eng<UU(K)>& c) {
  if constexpr (UU(K) == 8 && N % 3 == 0) __builtin_amdgcn_s_setprio((N % 9) / 3);
  eng_units<K, N>(c, std::make_integer_sequence<int, 2 * UU(K)>{}); }
template <int K, int P> DI void eng_pass(Eng<UU(K)>& c) {

  eng_chunk<K, 9 * P + 0>(c); eng_chunk<K, 9 * P + 1>(c); eng_chunk<K, 9 * P + 2>(c);
  eng_chunk<K, 9 * P + 3>(c); eng_chunk<K, 9 * P + 4>(c); eng_chunk<K, 9 * P + 5>(c);
  eng_chunk<K, 9 * P + 6>(c); eng_chunk<K, 9 * P + 7>(c); eng_chunk<K, 9 * P + 8>(c);
  if constexpr (UU(K) == 8) __builtin_amdgcn_s_setprio(3);

}
template <int K, int... Is> DI void eng_prologue_(Eng<UU(K)>& c, std::integer_sequence<int, Is...>) { (eng_event<K, -1, Is>(c), ...); }
template <int K> DI void eng_start(Eng<UU(K)>& c, const void* x, char* ws, LAS char* lds, int mt, int lane, int wid) {
  constexpr int U = UU(K);
  c.lane = lane;
  c.y.rf = mk_rsrc(ws + WS_F, F_BYTES);
  c.rx = mk_rsrc(x, 2048 * 1152);
  {
    int o = lane >> 3, swz = ((o >> 1) & 1) | ((wid & 1) << 1);
    c.wvoff = (unsigned)(16 * wid + o) * ROWB(U) + (unsigned)(((lane & 7) ^ (swz << 1)) * 16);
    int f = (o >> 1) & 1;
    unsigned rowb = (unsigned)((mt * 128 + wid * 32 + o) * 1152);
    c.xvoff[0] = rowb + (unsigned)(((lane & 7) ^ f) * 16);
    c.xvoff[1] = rowb + (unsigned)(((lane & 7) ^ f ^ 6) * 16);
  }
  c.m0w = (unsigned)(size_t)lds + (unsigned)wid * 1024u;
  c.m0x = (unsigned)(size_t)lds + (unsigned)(XOFF + wid * 8192);
  {
    LAS char* xb = lds + XOFF + wid * 8192;
    int g = lane >> 4, t16 = lane & 15, f = ((t16 >> 1) & 1) | ((t16 & 8) ? 6 : 0);
    c.xr[0] = xb + t16 * 128 + (((2 * g) ^ f) * 16);
    c.xr[1] = xb + t16 * 128 + (((2 * g + 1) ^ f) * 16);
  }
  eng_prologue_<K>(c, std::make_integer_sequence<int, npos<K>(-1)>{});
  {
    unsigned long long e;
    asm volatile("s_load_dwordx2 %0, %1, 0x0\n\ts_waitcnt lgkmcnt(0)" : "=s"(e) : "s"(ws + WS_F + F_EP) : "memory");
    const unsigned long long t = e * 6364136223846793005ull + 1442695040888963407ull;
    c.y.t0 = (unsigned)t; c.y.t1 = (unsigned)(t >> 32);
  }
  wait_vm<younger<K>(2, 0, 0, 0)>();
  eng_xrd<U, 0, 0>(c); eng_xrd<U, 0, 1>(c); eng_xrd<U, 0, 2>(c); eng_xrd<U, 0, 3>(c);
  bar();
  if (wid == 0) publish(c.y, F_ST + blockIdx.x * 8u, lane);
  eng_prime<U, 0>(c);
}

constexpr int BLK = 18432, NB = 21;
constexpr int AB = ATT_LAST ? 18 : 12, PB = ATT_LAST ? 12 : 15;
constexpr bool is_att(int b) { return b >= AB && b < AB + 3; }
constexpr int gstart(int b) { return b < 6 ? 0 : b < 12 ? 6 : is_att(b) ? AB : PB; }
constexpr int npos2(int B) { return B == AB + 2 ? 9 : 18; }
constexpr int POLLI(int B) { return npos2(B) - 9; }
constexpr int PRO2 = 12 + 12 + 3;
constexpr Ev ev2_at(int B, int I) {
  if (B < 0) return I < 12 ? Ev{1, I / 5, I % 5} : I < 24 ? Ev{2, (I - 12) / 4, (I - 12) % 4} : Ev{3, 0, I - 24};
  const int n = npos2(B);
  if ((I == n - 4 || I == n - 2) && B + 3 < NB) return Ev{1, B + 3, (I - (n - 4)) / 2};
  if ((I == 0 || I == 2 || I == 4) && B + 2 < NB && n == 18) return Ev{1, B + 2, 2 + I / 2};
  if (n == 9 && B + 2 < NB && (I == 0 || I == 6 || I == 8)) return Ev{1, B + 2, I == 0 ? 2 : I == 6 ? 3 : 4};
  if (B + 1 < NB && gstart(B + 1) == B + 1 && I > POLLI(B) && I <= POLLI(B) + 4) return Ev{2, B + 1, I - POLLI(B) - 1};
  if (B > 0 && gstart(B) == B) {
    if (I >= 5 && I < 9) return Ev{2, B + 1, I - 5};
    if (I >= 9 && I < 13 && !(B + 2 == AB + 2 && I >= 11)) return Ev{2, B + 2, I - 9};
  }
  if (B >= 6 && gstart(B) != B && B + 2 < NB && gstart(B + 2) == gstart(B) && I >= 5 && I < 9) return Ev{2, B + 2, I - 5};
  if (B < 3 && (I == 6 || I == 8 || I == 10 || I == 12)) return Ev{2, B + 3, (I - 6) / 2};
  if (B + 2 < NB && gstart(B + 2) == B + 2 && I == 13) return Ev{4, B + 2, 0};
  if (B == AB + 1 && (I == 1 || I == 3)) return Ev{7, 0, I / 2};
  if (B == OWN_B && I == 3) return Ev{9, 0, 0};
  return Ev{0, 0, 0};
}
constexpr int younger2(int kind, int id, int piece, int B, int I) {
  int cnt = 0; bool seen = false;
  for (int n = -1; n <= B; ++n)
    for (int i = 0; i < (n == B ? I : (n < 0 ? PRO2 : npos2(n))); ++i) {
      Ev e = ev2_at(n, i);
      if (e.kind == kind && e.id == id && e.piece == piece) { cnt = 0; seen = true; } else if (e.kind != 0) ++cnt;
    }
  return seen ? cnt : -1;
}
constexpr int ubase(int B) { int u = 0; for (int b = 0; b < B; ++b) u += npos2(b); return u; }
constexpr unsigned gmask(int G) { return G == 0 ? 0x9249u : G == 6 ? 0x12492u : G == AB ? 0xc0000u : 0x24924u; }
struct G2 {
  Sync y;
  rsrc_t rd, ro, rh, rg;
  unsigned l16, wv[5], m0w, wso, aso, hso, oso, goff, fvo, fso, lbit, own;
  int rot, lane, wid;
  LAS char* rb[2];
  u32x4 h[4][4];
  u32x2 fl;
  unsigned gt[2];
  f32x4 acc[2][9], aca[2][9];
  bf16x4 wf[PF + 1][2];
};
template <int B> DI unsigned unit_of(const G2& c) {
  constexpr int g = gstart(B), i = B - g, p = g == 0 ? 0 : g == 6 ? 1 : 2;
  const int k = i + c.rot;
  return (unsigned)((k >= 6 ? k - 6 : k) * 3 + p);
}
template <int B, int I, int STEP = 0> DI void g2_event(G2& c) {
  constexpr Ev e = ev2_at(B, I);
  if constexpr (STEP == 1) {
    if constexpr (e.kind == 1 && e.piece < 4) dma16s<1>(c.m0w + (unsigned)((e.id % 4) * BLK + e.piece * 1024), 0u, c.rd, 0u);
  } else if constexpr (e.kind == 1) {
    const unsigned m0v = c.m0w + (unsigned)((e.id % 4) * BLK + e.piece * 1024);
    unsigned vo = c.wv[e.piece], so;
    if constexpr (!is_att(e.id)) so = c.wso + unit_of<e.id>(c) * (64u * 1152u);
    else if constexpr (e.id < AB + 2) so = c.aso + (unsigned)((e.id - AB) * 72 * 1152) + (unsigned)c.wid * (18u * 1152u);
    else {
      const int n = 64 * e.piece + c.lane, r = n / 18;
      vo += (unsigned)((r >> 2) * 14 * 1152);
      so = c.wid < 2 ? c.aso + (unsigned)(16 * 1152) + (unsigned)c.wid * (72u * 1152u) : OOB;
    }
    if constexpr (e.piece < 4) dma16s<STEP>(m0v, vo, is_att(e.id) ? c.ro : c.rd, so); else dma16_half(m0v, vo, is_att(e.id) ? c.ro : c.rd, so);
  } else if constexpr (e.kind == 2) {
    unsigned so;
    if constexpr (!is_att(e.id)) so = c.hso + unit_of<e.id>(c) * 2048u + (unsigned)((e.piece >> 1) * 1024 + (e.piece & 1) * 144 * 1024);
    else so = c.oso + (unsigned)((2 * (e.id - AB) + (e.piece >> 1)) * 1024 + (e.piece & 1) * 20 * 1024);
    ld16_sc1(c.h[e.id % 4][e.piece], c.l16, c.rh, so);
  } else if constexpr (e.kind == 4) {
    ld8_sc1(c.fl, c.fvo, c.y.rf, c.fso);
  } else if constexpr (e.kind == 7) {
    ld4_sc1(c.gt[e.piece], c.goff, c.rg, (unsigned)(e.piece * 64));
  } else if constexpr (e.kind == 9) {
    wait_vm<younger2(3, 0, 2, B, I)>();
    publish(c.y, c.own, c.lane);
  }
}
template <int B, int I, int HALF> DI bf16x4 g2_rd(G2& c) {
  constexpr int o = (B % 4) * BLK + (I / 9) * 9216 + HALF * 4608 + (I % 9) * 32;
  return trd(c.rb[o / 36864] + o % 36864);
}
template <int B, int I, int HALF> DI void g2_pref(G2& c) {
  constexpr int W = (ubase(B) + I + PF) % (PF + 1);
  if constexpr (I + PF < npos2(B)) c.wf[W][HALF] = g2_rd<B, I + PF, HALF>(c);
  else if constexpr (B < NB - 1) c.wf[W][HALF] = g2_rd<B + 1, I + PF - npos2(B), HALF>(c);
}
template <int B, int I> DI void g2_unit(G2& c) {
  constexpr int ks = I / 9, u = I % 9, W = (ubase(B) + I) % (PF + 1);
  if constexpr (u == 0) {
    wait_vm<younger2(2, B, 2 * ks + 1, B, I)>();
    pin(c.h[B % 4][2 * ks]); pin(c.h[B % 4][2 * ks + 1]);
  }
  if constexpr (I == npos2(B) - PF && B < NB - 1) { wait_vm<younger2(1, B + 1, 4, B, I)>(); bar(); }
  bf16x8 w = cat(c.wf[W][0], c.wf[W][1]);
  const u32x4 b0 = c.h[B % 4][2 * ks], b1 = c.h[B % 4][2 * ks + 1];
  constexpr bool first = (B == 0 || B == AB) && ks == 0;
  f32x4& a0 = is_att(B) ? c.aca[0][u] : c.acc[0][u];
  f32x4& a1 = is_att(B) ? c.aca[1][u] : c.acc[1][u];
  if constexpr (first) mfma_zero(a0, w, b0); else mfma_acc(a0, w, b0);
  SB(); g2_pref<B, I, 0>(c); g2_event<B, I, 1>(c); SB();
  if constexpr (first) mfma_zero(a1, w, b1); else mfma_acc(a1, w, b1);
  SB(); g2_pref<B, I, 1>(c); SB();
  g2_event<B, I, 2>(c); SB();
  if constexpr (B + 1 < NB && gstart(B + 1) == B + 1 && I == POLLI(B)) {
    wait_vm<younger2(4, B + 1, 0, B, I + 1)>();
    pin(c.fl);
    poll(c.y, c.fl, (c.lbit & gmask(B + 1)) != 0u, c.fvo, c.fso);
    SB();
  }
}
template <int B, int... Is> DI void g2_units(G2& c, std::integer_sequence<int, Is...>) { (g2_unit<B, Is>(c), ...); }
template <int B> DI void g2_block(G2& c) { g2_units<B>(c, std::make_integer_sequence<int, npos2(B)>{}); }
template <int O, int... Is> DI void g2_pro(G2& c, std::integer_sequence<int, Is...>) { (g2_event<-1, O + Is>(c), ...); }
template <int... Is> DI void g2_prime(G2& c, std::integer_sequence<int, Is...>) { ((c.wf[Is >> 1][Is & 1] = g2_rd<0, (Is >> 1), (Is & 1)>(c)), ...); }
DI f32x4 up4(unsigned a, unsigned b) {
  return f32x4{__builtin_bit_cast(float, a << 16), __builtin_bit_cast(float, a & 0xffff0000u), __builtin_bit_cast(float, b << 16), __builtin_bit_cast(float, b & 0xffff0000u)};
}
DI void p2_begin(G2& c, const Sync& y, const void* wo, const void* wd, char* ws, LAS char* lds, int xcd, int ml, int ng) {
  const int lane = threadIdx.x & 63, wid = rfl(threadIdx.x >> 6);
  const int s = xcd >> 1, hm = xcd & 1, m = hm * 8 + ml;
  const int tq = wid, t16 = lane & 15, quad = lane >> 4;
  c.y = y; c.lane = lane; c.wid = wid;
  c.rd = mk_rsrc(wd, 4608 * 1152); c.ro = mk_rsrc(wo, 576 * 1152);
  c.rh = mk_rsrc(ws + WS_H, (unsigned)(H_BYTES + OB_BYTES)); c.rg = mk_rsrc(ws + WS_G, (unsigned)G_BYTES);
  c.l16 = (unsigned)lane * 16u;
#pragma unroll
  for (int i = 0; i < 5; ++i) { const int n = 64 * i + lane, r = n / 18; c.wv[i] = (unsigned)(r * 1152 + (n - 18 * r) * 16); }
  c.m0w = (unsigned)(size_t)lds + (unsigned)wid * 4608u;
  c.wso = (unsigned)((1152 * s + 16 * wid) * 1152 + 288 * ng);
  c.aso = (unsigned)(144 * s * 1152 + 288 * ng);
  c.hso = (unsigned)(((m * 8 + tq * 2) * 144 + 36 * s) * 1024);
  c.oso = (unsigned)H_BYTES + (unsigned)(((m * 8 + tq * 2) * 20 + 5 * s) * 1024);
  c.goff = (unsigned)((s * 2048 + m * 128 + tq * 32 + t16) * 4);
  c.fvo = lane < 20 ? (unsigned)lane * 8u : OOB;
  c.lbit = lane < 20 ? 1u << lane : 0u;
  c.fso = F_H + (unsigned)(((m * 4 + tq) * 4 + s) * 256);
  c.rot = (ml + (3 * ng) / 2) % 6;
  {
    const int q = (lane >> 2) & 3, p = lane & 3;
    c.rb[0] = lds + (4 * quad + q) * 288 + 8 * p; c.rb[1] = c.rb[0] + 36864;
    asm volatile("" : "+v"(c.rb[0]), "+v"(c.rb[1]));
  }
  ld8_sc1(c.fl, c.fvo, c.y.rf, c.fso);
  asm volatile("s_waitcnt lgkmcnt(0)" ::: "memory");
  bar();
  g2_pro<0>(c, std::make_integer_sequence<int, 12>{});
}
DI void p2_begin2(G2& c) {
  wait_vm<13>();
  pin(c.fl);
  poll(c.y, c.fl, (c.lbit & gmask(0)) != 0u, c.fvo, c.fso);
  g2_pro<12>(c, std::make_integer_sequence<int, 12>{});
}
DI void phase2(G2& c, char* ws, void* out, int xcd, int ml, int ng) {
  const int lane = c.lane, wid = c.wid;
  const int s = xcd >> 1, hm = xcd & 1, m = hm * 8 + ml;
  const int tq = wid, t16 = lane & 15, quad = lane >> 4, tile = m * 4 + ng;
  __builtin_amdgcn_s_setprio(P2_PRIO);
  wait_vm<younger2(1, 0, 4, 0, 0)>();
  bar();
  g2_prime(c, std::make_integer_sequence<int, 2 * PF>{});
  SB();

  g2_block<0>(c); g2_block<1>(c); g2_block<2>(c); g2_block<3>(c); g2_block<4>(c);

  g2_block<5>(c); g2_block<6>(c); g2_block<7>(c); g2_block<8>(c); g2_block<9>(c); g2_block<10>(c);

  g2_block<11>(c); g2_block<12>(c); g2_block<13>(c);

  g2_block<14>(c); g2_block<15>(c); g2_block<16>(c);

  g2_block<17>(c);

  g2_block<18>(c); g2_block<19>(c); g2_block<20>(c);
  wait_vm<0>();
  mfma_fence();
  pin(c.gt[0]); pin(c.gt[1]);
#pragma unroll
  for (int tt = 0; tt < 2; ++tt) {
    const float og = 1.0f - __builtin_bit_cast(float, c.gt[tt]);
#pragma unroll
    for (int k = 0; k < 9; ++k) c.acc[tt][k] = c.acc[tt][k] * og + c.aca[tt][k];
  }

  rsrc_t rxc = mk_rsrc(ws + WS_X, (unsigned)X_BYTES);
  const unsigned xo = (unsigned)(tile * 8 + tq * 2) * XUNIT;
  const bool own8 = tq == s;
  f32x4 r0[2], r1[2], r8[2];
#pragma unroll
  for (int tt = 0; tt < 2; ++tt) {
#pragma unroll
    for (int P = 0; P < 4; ++P) {
      const f32x4 p0 = c.acc[tt][2 * P], p1 = c.acc[tt][2 * P + 1];
      const u32x4 o = {pk2(p0[0], p0[1]), pk2(p0[2], p0[3]), pk2(p1[0], p1[1]), pk2(p1[2], p1[3])};
      st16(o, rxc, c.l16, P == s ? OOB : xo + (unsigned)(tt * XUNIT + (P * 3 + s - (s > P ? 1 : 0)) * 1024), 16);
    }
    const u32x2 o = {pk2(c.acc[tt][8][0], c.acc[tt][8][1]), pk2(c.acc[tt][8][2], c.acc[tt][8][3])};
    __builtin_amdgcn_raw_buffer_store_b64(o, rxc, (unsigned)lane * 8u, own8 ? OOB : xo + (unsigned)(tt * XUNIT + 12288 + (s - (s > tq ? 1 : 0)) * 512), 16);
    r0[tt] = c.acc[tt][0]; r1[tt] = c.acc[tt][1]; r8[tt] = c.acc[tt][8];
#pragma unroll
    for (int P = 1; P < 4; ++P) if (s == P) { r0[tt] = c.acc[tt][2 * P]; r1[tt] = c.acc[tt][2 * P + 1]; }
  }
  wait_vm<0>();
  const unsigned xf = F_X + (unsigned)((tile * 4 + tq) * 64);
  publish(c.y, xf + (unsigned)s * 8u, lane);

  {
    const u32x2 none = {~c.y.t0, 0u};
    unsigned spins = 0;
    u32x2 f = none;
    const unsigned fv = lane < 4 ? (unsigned)lane * 8u : OOB;
    do {
      asm volatile("buffer_load_dwordx2 %0, %1, %2, %3 offen sc1\n\ts_waitcnt vmcnt(0)" : "=&v"(f) : "v"(fv), "s"(c.y.rf), "s"(xf) : "memory");
    } while (flags_missing(c.y, f, lane < 4 && lane != s) && ++spins < SPIN_MAX);
  }

  {
    u32x4 pv[2][3]; u32x2 p8[2][3];
#pragma unroll
    for (int tt = 0; tt < 2; ++tt)
#pragma unroll
      for (int k = 0; k < 3; ++k) {
        ld16_sc1(pv[tt][k], c.l16, rxc, xo + (unsigned)(tt * XUNIT + (s * 3 + k) * 1024));
        ld8_sc1(p8[tt][k], (unsigned)lane * 8u, rxc, own8 ? xo + (unsigned)(tt * XUNIT + 12288 + k * 512) : OOB);
      }
    wait_vm<0>();
#pragma unroll
    for (int tt = 0; tt < 2; ++tt)
#pragma unroll
      for (int k = 0; k < 3; ++k) {
        pin(pv[tt][k]); pin(p8[tt][k]);
        r0[tt] += up4(pv[tt][k][0], pv[tt][k][1]); r1[tt] += up4(pv[tt][k][2], pv[tt][k][3]); r8[tt] += up4(p8[tt][k][0], p8[tt][k][1]);
      }
  }
#pragma unroll
  for (int tt = 0; tt < 2; ++tt) {
    char* orow = (char*)out + (size_t)(m * 128 + tq * 32 + tt * 16 + t16) * 1152 + ng * 288 + quad * 8;
    *(u32x2*)(orow + s * 64) = u32x2{pk2(r0[tt][0], r0[tt][1]), pk2(r0[tt][2], r0[tt][3])};
    *(u32x2*)(orow + s * 64 + 32) = u32x2{pk2(r1[tt][0], r1[tt][1]), pk2(r1[tt][2], r1[tt][3])};
    if (own8) *(u32x2*)(orow + 256) = u32x2{pk2(r8[tt][0], r8[tt][1]), pk2(r8[tt][2], r8[tt][3])};
  }

}

template <int NP, int P, int E> DI void ffn_egroup(Eng<8>& c, rsrc_t rh, unsigned hvoff, unsigned hsoff) {
  constexpr int tt = E >> 1, gi = E & 1;
  if constexpr (E == 0) mfma_fence();
  if constexpr (E == 3 && P < NP - 1) eng_prime<8, 9 * (P + 1)>(c);
  u32x4 o = gelu_mul8(c.acc[tt][2 * gi], c.acc[tt][2 * gi + 1], c.acc[tt][4 + 2 * gi], c.acc[tt][4 + 2 * gi + 1]);
  st16(o, rh, hvoff, hsoff + (unsigned)(P * 2048 + tt * 144 * 1024 + gi * 1024), 16);
  SB();
}
template <int NP, int P> DI void ffn_pass(Eng<8>& c, rsrc_t rh, unsigned hvoff, unsigned hsoff, rsrc_t rt, unsigned tv) {
  eng_pass<80 + NP, P>(c);
  if constexpr (P == TOUCH_P) { touch(c.dummy, tv, rt, 0u); SB(); }
  ffn_egroup<NP, P, 0>(c, rh, hvoff, hsoff); ffn_egroup<NP, P, 1>(c, rh, hvoff, hsoff);
  ffn_egroup<NP, P, 2>(c, rh, hvoff, hsoff); ffn_egroup<NP, P, 3>(c, rh, hvoff, hsoff);

}
DI void finish1(const Sync& y, unsigned own) {
  wait_vm<0>();
  publish(y, own, threadIdx.x & 63);
  if (blockIdx.x == 256) {
    const u32x2 none = {~y.t0, 0u};
    poll(y, none, true, threadIdx.x * 8u, F_ST);
    poll(y, none, true, threadIdx.x * 8u, F_ST + 2048u);
    __syncthreads();
    if (threadIdx.x < 64) publish(y, F_EP, threadIdx.x);
  }
}
DI void k1_ffn(const void* x, const void* wg, const void* wu, const void* wo, const void* wd, char* ws, void* out, LAS char* lds, int xcd, int idx) {
  const int lane = threadIdx.x & 63, wid = rfl(threadIdx.x >> 6);
  const int s = xcd >> 1, hm = xcd & 1, jj = idx % 6, ml = idx / 6, mt = hm * 8 + ml, un0 = 3 * jj;
  Eng<8> c;
  c.r0 = mk_rsrc(wg, 576 * 9216); c.r1 = mk_rsrc(wu, 576 * 9216);
  rsrc_t rh = mk_rsrc(ws + WS_H, (unsigned)H_BYTES);
  const unsigned hvoff = (unsigned)lane * 16u, hsoff = (unsigned)(((mt * 8 + wid * 2) * 144 + 36 * s + 2 * un0) * 1024);
  c.colb = (unsigned)(18 * s + un0) * 128u;
  c.foff = F_H + (unsigned)(((mt * 4 + wid) * 4 + s) * 256 + un0 * 8);
  {
    int g = lane >> 4, q = (lane >> 2) & 3, p = lane & 3, swz = (q >> 1) | ((g & 1) << 1);
#pragma unroll
    for (int i = 0; i < 4; ++i) { c.rb[i] = lds + 1024 * g + 128 * q + 8 * p + ((i ^ swz) * 32); asm volatile("" : "+v"(c.rb[i])); }
  }

  c.rot = ((0x41875420 >> (4 * ml) & 15) + (0x865320 >> (4 * jj) & 15)) % 9;
  if (idx >= 32) __builtin_amdgcn_s_sleep(S2_SLEEP);
  eng_start<83>(c, x, ws, lds, mt, lane, wid);
  c.dummy = 0;
  {
    rsrc_t rt = mk_rsrc(wd, 4608 * 1152);
    const unsigned tv = lane < 54 ? (unsigned)(1152 * s * 1152 + ((idx * 4 + wid) * 54 + lane) * 128) : OOB;
    ffn_pass<3, 0>(c, rh, hvoff, hsoff, rt, tv); ffn_pass<3, 1>(c, rh, hvoff, hsoff, rt, tv);
  }
  eng_pass<83, 2>(c);
  pin(c.dummy);
  const bool p2 = idx < 32;
  G2 d;
  if (p2) p2_begin(d, c.y, wo, wd, ws, lds, xcd, P2_M(idx), idx & 3);
  SB();
  ffn_egroup<3, 2, 0>(c, rh, hvoff, hsoff);
  if (p2) p2_begin2(d);
  SB();
  ffn_egroup<3, 2, 1>(c, rh, hvoff, hsoff); ffn_egroup<3, 2, 2>(c, rh, hvoff, hsoff); ffn_egroup<3, 2, 3>(c, rh, hvoff, hsoff);

  if (!p2) { finish1(c.y, c.foff + 16u); return; }
  d.own = c.foff + 16u;
  phase2(d, ws, out, xcd, P2_M(idx), idx & 3);
}

constexpr int KVP = 144;
template <int P> DI void aux_pack(Eng<5>& c, u32x2 (&o)[2][5]) {
  mfma_fence();
  if constexpr (P == 0) eng_prime<5, 9 * (P + 1)>(c);
#pragma unroll
  for (int tt = 0; tt < 2; ++tt)
#pragma unroll
    for (int t = 0; t < 5; ++t) {
      const f32x4 v = c.acc[tt][t];
      if constexpr (P != 1) o[tt][t] = u32x2{pk2(phi(v[0]), phi(v[1])), pk2(phi(v[2]), phi(v[3]))};
      else o[tt][t] = u32x2{pk2(v[0], v[1]), pk2(v[2], v[3])};
      pin(o[tt][t]);
    }
  mfma_fence();
}
DI void k1_aux(const void* x, const void* wqkv, const void* gw, char* ws, LAS char* lds, int xcd, int a) {
  const int lane = threadIdx.x & 63, wid = rfl(threadIdx.x >> 6);
  const int s = xcd >> 1, hm = xcd & 1, mt = hm * 8 + (a & 7), hp = a >> 3, hg = 2 * s + hp;
  const int g = lane >> 4, q = (lane >> 2) & 3, p = lane & 3, t16 = lane & 15;
  const int h = 4 * hg + wid, c0 = (18 * wid) & ~3, off = (18 * wid) & 3;
  Eng<5> c;
  c.r0 = mk_rsrc(wqkv, 576 * 3456); c.r1 = c.r0;
  c.rot = (a & 7) + hp;
  c.colb = (unsigned)hg * 128u;
  c.foff = F_K + (unsigned)(h * 128 + mt * 8);
  {
    int swz = (q >> 1) | ((g & 1) << 1);
#pragma unroll
    for (int t = 0; t < 5; ++t) {
      int b = 16 * hg + 32 * t + 8 * p; b = b > 248 ? 248 : b;
      c.rb[t] = lds + (b >> 7) * 4096 + 1024 * g + 128 * q + ((((b >> 5) & 3) ^ swz) * 32) + (b & 31);
      asm volatile("" : "+v"(c.rb[t]));
    }
  }

  __builtin_amdgcn_s_sleep(S2_SLEEP);
  __builtin_amdgcn_s_setprio(AUX_PRIO);
  eng_start<53>(c, x, ws, lds, mt, lane, wid);
  rsrc_t rkv = mk_rsrc(ws + WS_KV, (unsigned)KV_BYTES);
  u32x2 pk[2][5], pv[2][5];
  eng_pass<53, 0>(c);
  {
    rsrc_t rgw = mk_rsrc(gw, 1152);
    const unsigned m0g = (unsigned)(size_t)lds + (unsigned)(XOFF + 24576 + wid * 2048);
    dma16(m0g, (unsigned)lane * 16u, rgw, 0u); dma16(m0g + 1024u, (unsigned)lane * 16u, rgw, 1024u);
  }
  aux_pack<0>(c, pk);
  eng_pass<53, 1>(c); aux_pack<1>(c, pv);
  {
    LAS char* imgK = lds + XOFF; LAS char* imgV = lds + XOFF + 10240;
    f32x4 kv[2][2] = {}, z[2] = {};
    const u32x4 one4 = {0x3f803f80u, 0x3f803f80u, 0x3f803f80u, 0x3f803f80u};
    const bf16x8 ones = __builtin_bit_cast(bf16x8, one4);
    const int wo_ = (wid * 16 + t16) * KVP + g * 8, ro = (8 * g + q) * KVP + c0 * 2 + p * 8;
#pragma unroll
    for (int tt = 0; tt < 2; ++tt) {
#pragma unroll
      for (int t = 0; t < 5; ++t)
        if (t < 4 || g < 2) { *(LAS u32x2*)(imgK + wo_ + t * 32) = pk[tt][t]; *(LAS u32x2*)(imgV + wo_ + t * 32) = pv[tt][t]; }
      asm volatile("s_waitcnt lgkmcnt(0)" ::: "memory");
      bar();
#pragma unroll
      for (int ks = 0; ks < 2; ++ks) {
        bf16x8 fa[2], fb[2];
#pragma unroll
        for (int i = 0; i < 2; ++i) {
          fa[i] = cat(trd(imgK + ro + ks * 32 * KVP + i * 32), trd(imgK + ro + ks * 32 * KVP + i * 32 + 4 * KVP));
          fb[i] = cat(trd(imgV + ro + ks * 32 * KVP + i * 32), trd(imgV + ro + ks * 32 * KVP + i * 32 + 4 * KVP));
        }
#pragma unroll
        for (int i = 0; i < 2; ++i) {
          kv[i][0] = mfma(fa[i], fb[0], kv[i][0]); kv[i][1] = mfma(fa[i], fb[1], kv[i][1]); z[i] = mfma(fa[i], ones, z[i]);
        }
        SB();
      }
      asm volatile("s_waitcnt lgkmcnt(0)" ::: "memory");
      bar();
    }

    const unsigned so = (unsigned)((h * 16 + mt) * 2048);
#pragma unroll
    for (int i = 0; i < 2; ++i) {
      const bool dok = i == 0 || g == 0;
#pragma unroll
      for (int jb = 0; jb < 2; ++jb) {
        const int e = 16 * jb + t16 - off;
        const bool ok = dok && e >= 0 && e < 18;
        st16(__builtin_bit_cast(u32x4, kv[i][jb]), rkv, ok ? (unsigned)((e * 24 + 16 * i + 4 * g) * 4) : OOB, so, 16);
      }
      st16(__builtin_bit_cast(u32x4, z[i]), rkv, (dok && t16 == 0) ? (unsigned)((18 * 24 + 16 * i + 4 * g) * 4) : OOB, so, 16);
    }
    mfma_fence();
  }

  float gate[2];
  {
    f32x4 l0 = {0.f, 0.f, 0.f, 0.f}, l1 = {0.f, 0.f, 0.f, 0.f};
    LAS char* gl = lds + XOFF + 24576 + wid * 2048 + g * 32;
#pragma unroll
    for (int i = 0; i < 18; ++i) {
      const bf16x8 w = *(LAS bf16x8*)(gl + kc_of(c.rot, i >> 1) * 128 + (i & 1) * 16);
      l0 = mfma(w, __builtin_bit_cast(bf16x8, c.x[i >> 1][i & 1]), l0);
      l1 = mfma(w, __builtin_bit_cast(bf16x8, c.x[i >> 1][2 + (i & 1)]), l1);
      if (i % 6 == 5) SB();
    }
    gate[0] = 1.0f / (1.0f + __expf(-l0[0])); gate[1] = 1.0f / (1.0f + __expf(-l1[0]));
    asm volatile("" : "+v"(gate[0]), "+v"(gate[1]));
    mfma_fence();
  }
  eng_prime<5, 18>(c);

  eng_pass<53, 2>(c);
  u32x2 pq[2][5];
  LAS char* ka = lds + XOFF;
  {
    const u32x2 none = {~c.y.t0, 0u};
    poll(c.y, none, lane < 16, lane < 16 ? (unsigned)lane * 8u : OOB, F_K + (unsigned)h * 128u);

    f32x4 sa = {0.f, 0.f, 0.f, 0.f}, sb = {0.f, 0.f, 0.f, 0.f};
    const unsigned v0 = (unsigned)lane * 16u, v1 = lane < 50 ? v0 + 1024u : OOB;
    u32x4 la[16], lb[16];
#pragma unroll
    for (int i = 0; i < 16; ++i) { ld16_sc1(la[i], v0, rkv, (unsigned)((h * 16 + i) * 2048)); ld16_sc1(lb[i], v1, rkv, (unsigned)((h * 16 + i) * 2048)); }

    aux_pack<2>(c, pq);

    wait_vm<0>();

#pragma unroll
    for (int i = 0; i < 16; ++i) { pin(la[i]); pin(lb[i]); sa += __builtin_bit_cast(f32x4, la[i]); sb += __builtin_bit_cast(f32x4, lb[i]); }
    const int da = 4 * (lane % 6) - off, db = 4 * ((lane + 64) % 6) - off;
#pragma unroll
    for (int j = 0; j < 4; ++j) {
      sa[j] = (da + j >= 0 && da + j < 18) ? sa[j] : 0.f;
      sb[j] = (db + j >= 0 && db + j < 18 && lane < 50) ? sb[j] : 0.f;
    }
    *(LAS u32x2*)(ka + wid * 1024 + lane * 8) = u32x2{pk2(sa[0], sa[1]), pk2(sa[2], sa[3])};
    *(LAS u32x2*)(ka + wid * 1024 + 512 + lane * 8) = u32x2{pk2(sb[0], sb[1]), pk2(sb[2], sb[3])};
  }
  asm volatile("s_waitcnt lgkmcnt(0)" ::: "memory");
  bar();

  LAS char* iq = lds + wid * 5120;
#pragma unroll
  for (int tt = 0; tt < 2; ++tt)
#pragma unroll
    for (int t = 0; t < 5; ++t)
      if (t < 4 || g < 2) *(LAS u32x2*)(iq + (tt * 16 + t16) * KVP + t * 32 + g * 8) = pq[tt][t];
  rsrc_t rob = mk_rsrc(ws + WS_OB, (unsigned)OB_BYTES);
#pragma unroll
  for (int tt = 0; tt < 2; ++tt) {
    u32x4 ob[2]; float l0 = 0.f, l1 = 0.f;
#pragma unroll
    for (int hh = 0; hh < 4; ++hh) {
      constexpr int w0[4] = {0, 4, 9, 13};
      const int ca = w0[hh] + (g < 3 ? 2 * g : 0), cb = w0[hh] + (g < 2 ? 2 * g + 1 : 0);
      u32x2 ba = *(LAS u32x2*)(iq + (tt * 16 + t16) * KVP + ca * 8), bb = *(LAS u32x2*)(iq + (tt * 16 + t16) * KVP + cb * 8);
      const u32x4 bq = {ba[0], ba[1], bb[0], bb[1]};
      const int e1 = (t16 & 3) == 2 ? 18 : (((t16 >> 2) == hh && (t16 & 3) < 2) ? 16 + (t16 & 3) : 19);
      const bf16x8 a0 = *(LAS bf16x8*)(ka + hh * 1024 + (g < 3 ? t16 * 48 + g * 16 : 19 * 48));
      const bf16x8 a1 = *(LAS bf16x8*)(ka + hh * 1024 + (g < 3 ? e1 * 48 + g * 16 : 19 * 48));
      const f32x4 z = {0.f, 0.f, 0.f, 0.f};
      const f32x4 n1 = mfma(a0, __builtin_bit_cast(bf16x8, bq), z), n2 = mfma(a1, __builtin_bit_cast(bf16x8, bq), z);
      const float sc = gate[tt] * __builtin_amdgcn_rcpf(n2[2] + 1e-6f);
      ob[hh >> 1][2 * (hh & 1)] = pk2(n1[0] * sc, n1[1] * sc);
      ob[hh >> 1][2 * (hh & 1) + 1] = pk2(n1[2] * sc, n1[3] * sc);
      l0 += n2[0] * sc; l1 += n2[1] * sc;
    }
    const unsigned so = (unsigned)(((mt * 8 + wid * 2 + tt) * 20 + 5 * s) * 1024);
    st16(ob[0], rob, (unsigned)lane * 16u, so + (unsigned)(2 * hp) * 1024u, 16);
    st16(ob[1], rob, (unsigned)lane * 16u, so + (unsigned)(2 * hp + 1) * 1024u, 16);
    __builtin_amdgcn_raw_buffer_store_b64(u32x2{pk2(l0, l1), 0u}, rob, (unsigned)lane * 16u + (unsigned)hp * 8u, so + 4096u, 16);
  }
  {
    rsrc_t rg = mk_rsrc(ws + WS_G, (unsigned)G_BYTES);
    const float gl = (lane & 16) ? gate[1] : gate[0];
    __builtin_amdgcn_raw_buffer_store_b32(__builtin_bit_cast(unsigned, gl), rg, (hp == 0 && lane < 32) ? (unsigned)lane * 4u : OOB,
                                          (unsigned)((s * 2048 + mt * 128 + wid * 32) * 4), 16);
  }

  finish1(c.y, F_H + (unsigned)(((mt * 4 + wid) * 4 + s) * 256 + (18 + hp) * 8));
}
#endif

#if defined(__HIP_DEVICE_COMPILE__)
__attribute__((target("no-packed-fp32-ops")))
#endif
__global__ __launch_bounds__(256, 2) void fused(const void* x, const void* wqkv, const void* wo, const void* wg, const void* wu,
                                                const void* wd, const void* gw, char* ws, void* out) {
#if defined(__HIP_DEVICE_COMPILE__)
  extern __shared__ __attribute__((aligned(16))) char smem[];
  LAS char* lds = (LAS char*)smem;
  const int xcd = blockIdx.x & 7, idx = blockIdx.x >> 3;
  if (idx < 48) k1_ffn(x, wg, wu, wo, wd, ws, out, lds, xcd, idx);
  else k1_aux(x, wqkv, gw, ws, lds, xcd, idx - 48);
#endif
}

extern "C" void kernel_launch(void* const* d_in, const int* in_sizes, int n_in, void* d_out, int out_size, void* d_ws, size_t ws_size,
                              hipStream_t stream) {
  (void)hipFuncSetAttribute((const void*)fused, hipFuncAttributeMaxDynamicSharedMemorySize, K1_LDS);
  fused<<<512, 256, K1_LDS, stream>>>(d_in[0], d_in[1], d_in[2], d_in[4], d_in[5], d_in[6], d_in[7], (char*)d_ws, d_out);
}
```
